```python
import torch

def _softmax(x, axis=-1):
    return torch.softmax(x, dim=axis)
def _gelu(x):
    return torch.nn.functional.gelu(x, approximate="tanh")
def _silu(x):
    return torch.nn.functional.silu(x)
def _sigmoid(x):
    return torch.sigmoid(x)
def _layernorm(x, axis=-1, eps=1e-5):
    m = x.mean(dim=axis, keepdim=True)
    v = x.var(dim=axis, keepdim=True, unbiased=False)
    return (x - m) / torch.sqrt(v + eps)
def _scatter_add(tgt, idx, src, axis=0):
    return tgt.clone().index_add_(axis, idx, src)
def _segment_sum(src, seg, n):
    out = torch.zeros((n,) + src.shape[1:], dtype=src.dtype)
    return out.index_add_(0, seg, src)
def _pad(x, pw):
    flat = tuple(v for pair in reversed(pw) for v in pair)
    return torch.nn.functional.pad(x, flat)

kernel_name = 'recurrent_scan__gqa_block_cached__swiglu_ffn__decode'

def setup_inputs():

    _w = (lambda a: (lambda t: t.to(torch.bfloat16) if t.is_floating_point() else t)(torch.as_tensor(a)))
    def _A_setup():
        B, Sq, D = 1, 1, 576
        Hq, Hkv, Sk, Dh = 32, 16, 8192, 128
        assert Hq % Hkv == 0, (Hq, Hkv)
        return {
            "x": _w((lambda *s: torch.randn(*s, dtype=torch.bfloat16) * 0.05)(B, Sq, D)),
            "k_cache": _w((lambda *s: torch.randn(*s, dtype=torch.bfloat16) * 1.6)(B, Hkv, Sk, Dh)),
            "v_cache": _w((lambda *s: torch.randn(*s, dtype=torch.bfloat16) * 0.2)(B, Hkv, Sk, Dh)),
            "wq": _w((lambda *s: torch.randn(*s, dtype=torch.bfloat16) * 1.6)(D, Hq * Dh) * (D ** -0.5)),
            "wo": _w((lambda *s: torch.randn(*s, dtype=torch.bfloat16) * 0.2)(Hq * Dh, D) * ((Hq * Dh) ** -0.5)),
        }
    _A_in = _A_setup()

    def _B_setup():
        B, S, D, F = 1, 1, 576, 4608
        return {
            "x": _w((lambda *s: torch.randn(*s, dtype=torch.bfloat16) * 0.05)(B, S, D)),
            "w_gate": _w((lambda *s: torch.randn(*s, dtype=torch.bfloat16) * 0.1)(D, F)),
            "w_up": _w((lambda *s: torch.randn(*s, dtype=torch.bfloat16) * 0.1)(D, F)),
            "w_down": _w((lambda *s: torch.randn(*s, dtype=torch.bfloat16) * 0.1)(F, D)),
        }
    _B_in = _B_setup()

    return {
        "A_x": _A_in["x"],
        "A_k_cache": _A_in["k_cache"],
        "A_v_cache": _A_in["v_cache"],
        "A_wq": _A_in["wq"],
        "A_wo": _A_in["wo"],
        "B_x": _B_in["x"],
        "B_w_gate": _B_in["w_gate"],
        "B_w_up": _B_in["w_up"],
        "B_w_down": _B_in["w_down"],
    }

def reference(A_x, A_k_cache, A_v_cache, A_wq, A_wo, B_x, B_w_gate, B_w_up, B_w_down):
    def _A_ref(x, k_cache, v_cache, wq, wo):
        B, Sq, D = x.shape
        Hq, Dh = 32, 128
        Hkv = k_cache.shape[1]
        rep = Hq // Hkv
        q = (lambda x, *d: x.permute(*d))((x @ wq).reshape(B, Sq, Hq, Dh), 0, 2, 1, 3)
        k = (lambda x, n, axis: torch.repeat_interleave(x, n, dim=axis))(k_cache, rep, axis=1)
        v = (lambda x, n, axis: torch.repeat_interleave(x, n, dim=axis))(v_cache, rep, axis=1)
        scores = torch.einsum("bhqd,bhkd->bhqk", q, k) * (Dh ** -0.5)
        p = _softmax(scores, axis=-1)
        o = torch.einsum("bhqk,bhkd->bhqd", p, v)
        return (lambda x, *d: x.permute(*d))(o, 0, 2, 1, 3).reshape(B, Sq, Hq * Dh) @ wo

    def _B_ref(x, w_gate, w_up, w_down):
        g = _silu(x @ w_gate)
        u = x @ w_up
        return (g * u) @ w_down

    x = A_x
    xn = _layernorm(x, axis=-1)
    s = _A_ref(xn, A_k_cache, A_v_cache, A_wq, A_wo)
    g = _silu(_B_ref(xn, B_w_gate, B_w_up, B_w_down))
    return x + s * g
```

```cpp
#include <hip/hip_runtime.h>
#include <hip/hip_bf16.h>
#include <stdint.h>

typedef unsigned short u16;
constexpr int D = 576, HQ = 32, HKV = 16, SK = 8192, DH = 128, F = 4608;
constexpr int NSPLIT = 16, KEYS = SK / NSPLIT;

__device__ __forceinline__ float bf2f(u16 v) { return __builtin_bit_cast(float, (unsigned)v << 16); }
__device__ __forceinline__ float rbf(float f) { return __bfloat162float(__float2bfloat16(f)); }

__device__ void layernorm_to_lds(const u16* x, float* xn, float* red) {
  int t = threadIdx.x;
  float s = 0.f;
  for (int i = t; i < D; i += blockDim.x) s += bf2f(x[i]);
  for (int o = 32; o; o >>= 1) s += __shfl_xor(s, o);
  if ((t & 63) == 0) red[t >> 6] = s;
  __syncthreads();
  float tot = 0.f;
  for (int w = 0; w < (int)(blockDim.x >> 6); ++w) tot += red[w];
  float mean = tot / D;
  __syncthreads();
  float v = 0.f;
  for (int i = t; i < D; i += blockDim.x) { float d = bf2f(x[i]) - mean; v += d * d; }
  for (int o = 32; o; o >>= 1) v += __shfl_xor(v, o);
  if ((t & 63) == 0) red[t >> 6] = v;
  __syncthreads();
  float vt = 0.f;
  for (int w = 0; w < (int)(blockDim.x >> 6); ++w) vt += red[w];
  float var = rbf(vt / D);
  float m = rbf(mean);
  float den = rbf(sqrtf(rbf(var + 1e-5f)));
  for (int i = t; i < D; i += blockDim.x) xn[i] = rbf(rbf(bf2f(x[i]) - m) / den);
  __syncthreads();
}

__global__ void k1(const u16* x, const u16* wq, const u16* wg, const u16* wu, float* q, float* h) {
  __shared__ float xn[D];
  __shared__ float red[16];
  __shared__ float part[2][4][64];
  layernorm_to_lds(x, xn, red);
  int lane = threadIdx.x & 63, w = threadIdx.x >> 6;
  int cb = blockIdx.x;
  if (cb < 64) {
    int c = cb * 64 + lane;
    float a = 0.f;
    for (int r = w; r < D; r += 4) a += xn[r] * bf2f(wq[(size_t)r * 4096 + c]);
    part[0][w][lane] = a;
    __syncthreads();
    if (w == 0) q[c] = rbf(part[0][0][lane] + part[0][1][lane] + part[0][2][lane] + part[0][3][lane]);
  } else {
    int c = (cb - 64) * 64 + lane;
    float a = 0.f, b = 0.f;
    for (int r = w; r < D; r += 4) { a += xn[r] * bf2f(wg[(size_t)r * F + c]); b += xn[r] * bf2f(wu[(size_t)r * F + c]); }
    part[0][w][lane] = a; part[1][w][lane] = b;
    __syncthreads();
    if (w == 0) {
      float g = rbf(part[0][0][lane] + part[0][1][lane] + part[0][2][lane] + part[0][3][lane]);
      float u = rbf(part[1][0][lane] + part[1][1][lane] + part[1][2][lane] + part[1][3][lane]);
      float sg = rbf(g / (1.f + __expf(-g)));
      h[c] = rbf(sg * u);
    }
  }
}

__global__ void k2(const u16* K, const u16* V, const float* q, float* pm, float* pl, float* pacc) {
  __shared__ float qs[2][DH];
  __shared__ float sc[2][KEYS];
  __shared__ float red[2][4];
  __shared__ float accs[4][2][DH];
  int kv = blockIdx.x / NSPLIT, sp = blockIdx.x % NSPLIT;
  int t = threadIdx.x, lane = t & 63, w = t >> 6;
  qs[t >> 7][t & 127] = q[kv * 256 + t];
  __syncthreads();
  const u16* Kb = K + ((size_t)kv * SK + sp * KEYS) * DH;
  const u16* Vb = V + ((size_t)kv * SK + sp * KEYS) * DH;
  for (int k = w * 4 + (lane >> 4); k < KEYS; k += 16) {
    uint4 kk = *(const uint4*)(Kb + (size_t)k * DH + (lane & 15) * 8);
    unsigned a[4] = {kk.x, kk.y, kk.z, kk.w};
    float s0 = 0.f, s1 = 0.f;
    for (int j = 0; j < 4; ++j) {
      float lo = __builtin_bit_cast(float, a[j] << 16), hi = __builtin_bit_cast(float, a[j] & 0xffff0000u);
      int d = (lane & 15) * 8 + 2 * j;
      s0 += lo * qs[0][d] + hi * qs[0][d + 1];
      s1 += lo * qs[1][d] + hi * qs[1][d + 1];
    }
    for (int o = 8; o; o >>= 1) { s0 += __shfl_xor(s0, o); s1 += __shfl_xor(s1, o); }
    if ((lane & 15) == 0) { sc[0][k] = s0 * 0.088388347648318440f; sc[1][k] = s1 * 0.088388347648318440f; }
  }
  __syncthreads();
  float m0 = -INFINITY, m1 = -INFINITY;
  for (int k = t; k < KEYS; k += 256) { m0 = fmaxf(m0, sc[0][k]); m1 = fmaxf(m1, sc[1][k]); }
  for (int o = 32; o; o >>= 1) { m0 = fmaxf(m0, __shfl_xor(m0, o)); m1 = fmaxf(m1, __shfl_xor(m1, o)); }
  if (lane == 0) { red[0][w] = m0; red[1][w] = m1; }
  __syncthreads();
  m0 = fmaxf(fmaxf(red[0][0], red[0][1]), fmaxf(red[0][2], red[0][3]));
  m1 = fmaxf(fmaxf(red[1][0], red[1][1]), fmaxf(red[1][2], red[1][3]));
  __syncthreads();
  float l0 = 0.f, l1 = 0.f;
  for (int k = t; k < KEYS; k += 256) {
    float p0 = __expf(sc[0][k] - m0), p1 = __expf(sc[1][k] - m1);
    sc[0][k] = p0; sc[1][k] = p1; l0 += p0; l1 += p1;
  }
  for (int o = 32; o; o >>= 1) { l0 += __shfl_xor(l0, o); l1 += __shfl_xor(l1, o); }
  if (lane == 0) { red[0][w] = l0; red[1][w] = l1; }
  __syncthreads();
  l0 = red[0][0] + red[0][1] + red[0][2] + red[0][3];
  l1 = red[1][0] + red[1][1] + red[1][2] + red[1][3];
  float a0[2] = {0.f, 0.f}, a1[2] = {0.f, 0.f};
  for (int k = w; k < KEYS; k += 4) {
    unsigned vv = *(const unsigned*)(Vb + (size_t)k * DH + lane * 2);
    float lo = __builtin_bit_cast(float, vv << 16), hi = __builtin_bit_cast(float, vv & 0xffff0000u);
    a0[0] += sc[0][k] * lo; a0[1] += sc[0][k] * hi;
    a1[0] += sc[1][k] * lo; a1[1] += sc[1][k] * hi;
  }
  accs[w][0][lane * 2] = a0[0]; accs[w][0][lane * 2 + 1] = a0[1];
  accs[w][1][lane * 2] = a1[0]; accs[w][1][lane * 2 + 1] = a1[1];
  __syncthreads();
  int hh = t >> 7, d = t & 127;
  float tot = accs[0][hh][d] + accs[1][hh][d] + accs[2][hh][d] + accs[3][hh][d];
  pacc[((size_t)(kv * 2 + hh) * NSPLIT + sp) * DH + d] = tot;
  if (t == 0) { pm[(kv * 2) * NSPLIT + sp] = m0; pm[(kv * 2 + 1) * NSPLIT + sp] = m1; pl[(kv * 2) * NSPLIT + sp] = l0; pl[(kv * 2 + 1) * NSPLIT + sp] = l1; }
}

__global__ void k3(const u16* wo, const u16* wd, const float* pm, const float* pl, const float* pacc, const float* h, float* part) {
  __shared__ float o[16];
  int b = blockIdx.x, t = threadIdx.x;
  const u16* W;
  if (b < 256) {
    int hd = b >> 3, d0 = (b & 7) * 16;
    if (t < 16) {
      float M = -INFINITY;
      for (int s = 0; s < NSPLIT; ++s) M = fmaxf(M, pm[hd * NSPLIT + s]);
      float L = 0.f, a = 0.f;
      for (int s = 0; s < NSPLIT; ++s) { float e = __expf(pm[hd * NSPLIT + s] - M); L += e * pl[hd * NSPLIT + s]; a += e * pacc[((size_t)hd * NSPLIT + s) * DH + d0 + t]; }
      o[t] = rbf(a / L);
    }
    W = wo + (size_t)b * 16 * D;
  } else {
    if (t < 16) o[t] = h[(b - 256) * 16 + t];
    W = wd + (size_t)(b - 256) * 16 * D;
  }
  __syncthreads();
  for (int c = t; c < D; c += blockDim.x) {
    float a = 0.f;
    for (int r = 0; r < 16; ++r) a += o[r] * bf2f(W[r * D + c]);
    part[(size_t)b * D + c] = a;
  }
}

__global__ void k4(const u16* x, const float* part, __hip_bfloat16* out) {
  __shared__ float rs[4][64], rf[4][64];
  int lane = threadIdx.x & 63, w = threadIdx.x >> 6, c = blockIdx.x * 64 + lane;
  float s = 0.f, f = 0.f;
  for (int b = w; b < 256; b += 4) s += part[(size_t)b * D + c];
  for (int b = 256 + w; b < 256 + 288; b += 4) f += part[(size_t)b * D + c];
  rs[w][lane] = s; rf[w][lane] = f;
  __syncthreads();
  if (w == 0) {
    s = rbf(rs[0][lane] + rs[1][lane] + rs[2][lane] + rs[3][lane]);
    f = rbf(rf[0][lane] + rf[1][lane] + rf[2][lane] + rf[3][lane]);
    float g = rbf(f / (1.f + __expf(-f)));
    out[c] = __float2bfloat16(bf2f(x[c]) + rbf(s * g));
  }
}

extern "C" void kernel_launch(void* const* d_in, const int* in_sizes, int n_in,
                              void* d_out, int out_size, void* d_ws, size_t ws_size,
                              hipStream_t stream) {
  const u16* x = (const u16*)d_in[0];
  const u16* K = (const u16*)d_in[1];
  const u16* V = (const u16*)d_in[2];
  const u16* wq = (const u16*)d_in[3];
  const u16* wo = (const u16*)d_in[4];
  const u16* wg = (const u16*)d_in[6];
  const u16* wu = (const u16*)d_in[7];
  const u16* wd = (const u16*)d_in[8];
  float* ws = (float*)d_ws;
  float* q = ws;
  float* h = ws + 4096;
  float* pm = ws + 16384;
  float* pl = pm + 512;
  float* pacc = ws + 32768;
  float* part = ws + 32768 + 65536;
  k1<<<64 + 72, 256, 0, stream>>>(x, wq, wg, wu, q, h);
  k2<<<256, 256, 0, stream>>>(K, V, q, pm, pl, pacc);
  k3<<<256 + 288, 192, 0, stream>>>(wo, wd, pm, pl, pacc, h, part);
  k4<<<9, 256, 0, stream>>>(x, part, (__hip_bfloat16*)d_out);
}
```

```cpp
#include <hip/hip_runtime.h>
#include <hip/hip_bf16.h>
#include <stdint.h>
#include <type_traits>

#define NA 14
#define NB 19
#define WIN 8

typedef unsigned v4u __attribute__((ext_vector_type(4)));
typedef float v4f __attribute__((ext_vector_type(4)));
typedef __bf16 bf16x2 __attribute__((ext_vector_type(2)));
typedef __bf16 bf16x8 __attribute__((ext_vector_type(8)));
#define DI __device__ __forceinline__

constexpr int D = 576, SK = 8192, F = 4608;
constexpr int QUADS = SK / 4;
constexpr int QB = QUADS - 72 * NA;
constexpr int NFULL = QB - 56 * (NB - 1);
static_assert(NFULL > 0 && NFULL <= 56, "key split");

constexpr unsigned WS_EPOCH = 0;
constexpr unsigned WS_QP = 4096;
constexpr unsigned WS_HP = WS_QP + 16 * 16 * 512;
constexpr unsigned WS_ML = WS_HP + 144 * 1024;
constexpr unsigned WS_ACC = WS_ML + 4096;
constexpr unsigned WS_SF = WS_ACC + 256 * 1024;
constexpr int SPIN_CAP = 1 << 16;

struct Params {
  const char* x; const char* k; const char* v; const char* wq; const char* wo;
  const char* wg; const char* wu; const char* wd; char* ws; char* out;
};

template <int U, int N, class Fn> DI void sfor(Fn&& f) { if constexpr (U < N) { f(std::integral_constant<int, U>{}); sfor<U + 1, N>(f); } }
DI float asf(unsigned u) { return __builtin_bit_cast(float, u); }
DI unsigned asu(float f) { return __builtin_bit_cast(unsigned, f); }
DI unsigned pk(float lo, float hi) { bf16x2 r = {(__bf16)lo, (__bf16)hi}; return __builtin_bit_cast(unsigned, r); }
DI float rbf(float f) { return asf(pk(f, f) & 0xffff0000u); }
DI float dot2(unsigned a, unsigned b, float c) { return __builtin_amdgcn_fdot2_f32_bf16(__builtin_bit_cast(bf16x2, a), __builtin_bit_cast(bf16x2, b), c, false); }
template <int C> DI float dpp(float x) { return asf(__builtin_amdgcn_mov_dpp((int)asu(x), C, 0xf, 0xf, true)); }
constexpr int XOR1 = 0xB1, XOR2 = 0x4E, XOR7 = 0x141, XOR8 = 0x128;
DI float x16sum(float x) { auto s = __builtin_amdgcn_permlane16_swap(asu(x), asu(x), false, false); return asf(s[0]) + asf(s[1]); }
DI float x32sum(float x) { auto s = __builtin_amdgcn_permlane32_swap(asu(x), asu(x), false, false); return asf(s[0]) + asf(s[1]); }
DI float x16max(float x) { auto s = __builtin_amdgcn_permlane16_swap(asu(x), asu(x), false, false); return fmaxf(asf(s[0]), asf(s[1])); }
DI float x32max(float x) { auto s = __builtin_amdgcn_permlane32_swap(asu(x), asu(x), false, false); return fmaxf(asf(s[0]), asf(s[1])); }
DI float sum8(float x) { x += dpp<XOR1>(x); x += dpp<XOR2>(x); return x + dpp<XOR7>(x); }
DI float max8(float x) { x = fmaxf(x, dpp<XOR1>(x)); x = fmaxf(x, dpp<XOR2>(x)); return fmaxf(x, dpp<XOR7>(x)); }
DI float wavesum(float x) { x = sum8(x); x += dpp<XOR8>(x); return x32sum(x16sum(x)); }
DI float silu(float x) { return x * __builtin_amdgcn_rcpf(1.f + __expf(-x)); }
DI void fence_sched() { __builtin_amdgcn_sched_barrier(0); }
DI void wg_barrier() { asm volatile("s_waitcnt lgkmcnt(0)\n\ts_barrier" ::: "memory"); }

template <int OFF> DI void ld_nt(v4u& r, unsigned voff, const char* base) {
  asm volatile("global_load_dwordx4 %0, %1, %2 offset:%3 nt" : "=v"(r) : "v"(voff), "s"(base), "n"(OFF));
}
template <int OFF> DI void ld_dev(v4u& r, unsigned voff, const char* base) {
  asm volatile("global_load_dwordx4 %0, %1, %2 offset:%3 sc1" : "=v"(r) : "v"(voff), "s"(base), "n"(OFF));
}
DI void st_dev(unsigned voff, v4u d, char* base) {
  asm volatile("global_store_dwordx4 %0, %1, %2 sc1\n\ts_nop 1" :: "v"(voff), "v"(d), "s"(base) : "memory");
}
template <int N> DI void land(v4u& r) { asm volatile("s_waitcnt vmcnt(%1)" : "+v"(r) : "n"(N)); }
DI void pin(v4u& r) { asm volatile("" : "+v"(r)); }
DI v4u tagged(v4f v, unsigned tag) {
  v4u r; for (int i = 0; i < 4; ++i) r[i] = (asu(v[i]) & 0xffffff00u) | tag; return r;
}
DI v4f untag(v4u v) { v4f r; for (int i = 0; i < 4; ++i) r[i] = asf(v[i] & 0xffffff00u); return r; }
DI unsigned tagdiff(v4u v, unsigned tag) { return ((v[0] ^ tag) | (v[1] ^ tag) | (v[2] ^ tag) | (v[3] ^ tag)) & 0xffu; }
DI bool all_ok(unsigned diff) { return __builtin_amdgcn_ballot_w64(diff != 0) == 0; }
DI void repoll1(v4u& a, unsigned oa, const char* ws, unsigned tag) {
  #pragma unroll 1
  for (int s = 0; s < SPIN_CAP && !all_ok(tagdiff(a, tag)); ++s) { ld_dev<0>(a, oa, ws); land<0>(a); }
}
DI void repoll2(v4u& a, v4u& b, unsigned oa, unsigned ob, const char* ws, unsigned tag) {
  #pragma unroll 1
  for (int s = 0; s < SPIN_CAP && !all_ok(tagdiff(a, tag) | tagdiff(b, tag)); ++s) {
    ld_dev<0>(a, oa, ws); ld_dev<0>(b, ob, ws); land<0>(a); land<0>(b);
  }
}
DI v4f mac(v4f acc, unsigned dup, const v4u& msk, const v4u& w) {
  v4u a = {dup & msk[0], dup & msk[1], dup & msk[2], dup & msk[3]};
  return __builtin_amdgcn_mfma_f32_16x16x32_bf16(__builtin_bit_cast(bf16x8, a), __builtin_bit_cast(bf16x8, w), acc, 0, 0, 0);
}

struct Lds {
  unsigned xn[D];
  unsigned xraw[D / 2];
  float rq[8][128];
  float rg[8][128];
  float rqg[8][256];
  unsigned qb[128];
  float mx[2][8];
  float ls[2][8];
  float ra[8][256];
  float rs[8][2][2][64];
};

#define STAMP(i) do {} while (0)

template <bool A> DI void body(const Params& p, Lds& L, const int lane, const int w, const int g, const int j, const int aidx) {
  constexpr int NK = A ? NA : NB;
  constexpr int iX = 2, iWQ = 4, iK = 7, iQG = iK + NK, iGU = iQG + 1, iV = A ? iGU + 9 : iQG + 1;
  constexpr int iWD = iV + NK, iHG = iWD + 5, iML = iHG + 2, iWO = iML + 2, NOPS = A ? iWO + 4 : iV + NK;
  constexpr int NG = (NK + 7) / 8;
  STAMP(0);
  const int c = lane & 15, g4 = lane >> 4, r8 = lane >> 3, c8 = lane & 7;
  v4u r[NOPS];

  int quad0, nvalid;
  if (A) { quad0 = (j * 8 + w) * NA; nvalid = NA; }
  else { const int idx = w * 7 + (j - 9); quad0 = 72 * NA + idx * (NB - 1) + (idx < NFULL ? idx : NFULL); nvalid = idx < NFULL ? NB : NB - 1; }
  const unsigned kvrow0 = (unsigned)(g * SK + quad0 * 4) * 256u;
  const unsigned okv = kvrow0 + lane * 16;
  const unsigned okv_last = nvalid == NK ? okv + (NK - 1) * 1024 : kvrow0;
  const int wqrow0 = 72 * (j >> 1) + 9 * w;
  const unsigned owq = (unsigned)(wqrow0 + g4) * 8192u + ((2 * g + (j & 1)) * 128 + 8 * c) * 2;
  const unsigned owq2 = (unsigned)(wqrow0 + 8) * 8192u + ((2 * g + (j & 1)) * 128 + 8 * c) * 2;
  const unsigned oqg = WS_QP + (unsigned)((g * 16 + 2 * w) * 512 + lane * 16);
  const int gurow0 = 144 * (aidx & 3) + 36 * (w & 3);
  const unsigned ogu = (unsigned)(gurow0 + g4) * (F * 2) + ((aidx >> 2) * 128 + 8 * c) * 2;
  const char* wgu = w < 4 ? p.wg : p.wu;
  const unsigned owd = (unsigned)(288 * g + 36 * w + 4 * r8) * (D * 2) + (64 * j + 8 * c8) * 2;
  const unsigned owd4 = (unsigned)(288 * g + 36 * w + 32 + (r8 & 3)) * (D * 2) + (64 * j + 8 * c8) * 2;
  const unsigned owo = (unsigned)(256 * g + 32 * w + 4 * r8) * (D * 2) + (64 * j + 8 * c8) * 2;
  auto hpoff = [&](int col) { return WS_HP + (unsigned)((((col >> 7) * 4 + (lane & 3)) * 2 + ((lane >> 2) & 1)) * 512 + (col & 127) * 4); };
  const unsigned ohg0 = hpoff(288 * g + 36 * w + 4 * r8), ohg1 = hpoff(288 * g + 36 * w + 32);
  const unsigned oml0 = WS_ML + (unsigned)((g * 16 + c8) * 16);

  unsigned junk, epoch;
  auto issue = [&](auto N_) {
    constexpr int n = N_;
    if constexpr (n == 0) {
      unsigned long long pc; unsigned len;
      if constexpr (A) asm volatile("s_getpc_b64 %0\n.LcodeA0:\n\ts_mov_b32 %1, .LcodeA1-.LcodeA0" : "=s"(pc), "=s"(len));
      else asm volatile("s_getpc_b64 %0\n.LcodeB0:\n\ts_mov_b32 %1, .LcodeB1-.LcodeB0" : "=s"(pc), "=s"(len));
      const unsigned o = min((unsigned)(w * 64 + lane) * 128u, len);
      asm volatile("global_load_dword %0, %1, %2" : "=v"(junk) : "v"(o), "s"(pc));
    } else if constexpr (n == 1) {
      asm volatile("global_load_dword %0, %1, %2 sc1" : "=v"(epoch) : "v"(WS_EPOCH), "s"(p.ws));
    } else if constexpr (n == iX) {
      asm volatile("global_load_dwordx4 %0, %1, %2" : "=v"(r[n]) : "v"(lane * 16), "s"(p.x));
    } else if constexpr (n == iX + 1) {
      asm volatile("global_load_dwordx4 %0, %1, %2 offset:1024" : "=v"(r[n]) : "v"(c8 * 16), "s"(p.x));
    } else if constexpr (n < iK) {
      constexpr int i = n - iWQ;
      ld_nt<0>(r[n], i == 2 ? owq2 : owq + i * 4 * 8192, p.wq);
    } else if constexpr (n < iQG || (n >= iV && n < iV + NK)) {
      constexpr bool isk = n < iQG;
      constexpr int i = isk ? n - iK : n - iV;
      const char* base = isk ? p.k : p.v;
      if constexpr (i == NK - 1) ld_nt<0>(r[n], okv_last, base);
      else ld_nt<(i & 3) * 1024>(r[n], okv + (i >> 2) * 4096, base);
    } else if constexpr (n == iQG) {
      ld_dev<0>(r[n], oqg, p.ws);
    } else if constexpr (n < iV) {
      ld_nt<0>(r[n], ogu + (n - iGU) * 4 * (F * 2), wgu);
    } else if constexpr (n < iHG) {
      constexpr int i = n - iWD;
      if constexpr (i == 4) ld_nt<0>(r[n], owd4, p.wd); else ld_nt<i * D * 2>(r[n], owd, p.wd);
    } else if constexpr (n < iML) {
      ld_dev<0>(r[n], n == iHG ? ohg0 : ohg1, p.ws);
    } else if constexpr (n < iWO) {
      ld_dev<(n - iML) * 128>(r[n], oml0, p.ws);
    } else {
      ld_nt<(n - iWO) * D * 2>(r[n], owo, p.wo);
    }
  };
  auto step = [&](auto K_) {
    constexpr int k = K_;
    constexpr int issued = (k + WIN < NOPS) ? k + WIN : NOPS;
    land<issued - 1 - k>(r[k]);
    if constexpr (k + WIN < NOPS) issue(std::integral_constant<int, k + WIN>{});
  };

  sfor<0, iWQ + WIN>(issue);
  v4u msk;
  for (int i = 0; i < 4; ++i) msk[i] = (i == (c8 >> 1)) ? ((lane & 1) ? 0xffff0000u : 0x0000ffffu) : 0u;

  land<WIN>(r[iX + 1]);
  asm volatile("" : "+v"(r[iX]), "+v"(epoch), "+v"(junk));
  STAMP(1);
  const unsigned tag = (epoch + 1) & 0xffu;
  {
    float xv[16];
    for (int i = 0; i < 4; ++i) {
      xv[2 * i] = asf(r[iX][i] << 16); xv[2 * i + 1] = asf(r[iX][i] & 0xffff0000u);
      xv[8 + 2 * i] = asf(r[iX + 1][i] << 16); xv[9 + 2 * i] = asf(r[iX + 1][i] & 0xffff0000u);
    }
    const bool ext = lane < 8;
    float s0 = 0.f, s1 = 0.f;
    for (int i = 0; i < 8; ++i) { s0 += xv[i]; s1 += xv[8 + i]; }
    const float mean = wavesum(s0 + (ext ? s1 : 0.f)) * (1.f / D);
    s0 = 0.f; s1 = 0.f;
    for (int i = 0; i < 8; ++i) { const float a = xv[i] - mean, b = xv[8 + i] - mean; s0 += a * a; s1 += b * b; }
    const float var = rbf(wavesum(s0 + (ext ? s1 : 0.f)) * (1.f / D));
    const float mb = rbf(mean), den = rbf(__builtin_sqrtf(rbf(var + 1e-5f)));
    v4u o[4];
    for (int i = 0; i < 16; ++i) { const float y = rbf(xv[i] - mb) / den; o[i >> 2][i & 3] = pk(y, y); }
    *(v4u*)&L.xn[lane * 8] = o[0]; *(v4u*)&L.xn[lane * 8 + 4] = o[1];
    *(v4u*)&L.xn[512 + c8 * 8] = o[2]; *(v4u*)&L.xn[512 + c8 * 8 + 4] = o[3];
    if (!A) { *(v4u*)&L.xraw[lane * 4] = r[iX]; *(v4u*)&L.xraw[256 + c8 * 4] = r[iX + 1]; }
  }
  {
    const unsigned x0 = L.xn[wqrow0 + g4], x1 = L.xn[wqrow0 + 4 + g4], x2 = g4 == 0 ? L.xn[wqrow0 + 8] : 0u;
    sfor<iWQ, iK>(step);
    v4f a = {0.f, 0.f, 0.f, 0.f};
    a = mac(a, x0, msk, r[iWQ]); a = mac(a, x1, msk, r[iWQ + 1]); a = mac(a, x2, msk, r[iWQ + 2]);
    if (lane < 32) *(v4f*)&L.rq[w][8 * c + 4 * g4] = a;
    wg_barrier();
    if (w == 0 && lane < 32) {
      v4f s = *(v4f*)&L.rq[0][4 * lane];
      for (int i = 1; i < 8; ++i) s += *(v4f*)&L.rq[i][4 * lane];
      st_dev(WS_QP + (unsigned)((g * 16 + j) * 512 + lane * 16), tagged(s, tag), p.ws);
    }
    fence_sched();
  }
  STAMP(2);
  sfor<iK, iQG + 1>(step);
  STAMP(3);
  v4u qa, qb;
  {
    repoll1(r[iQG], oqg, p.ws, tag);
    *(v4f*)&L.rqg[w][4 * lane] = untag(r[iQG]);
    wg_barrier();
    v4f s = *(v4f*)&L.rqg[0][4 * lane];
    for (int i = 1; i < 8; ++i) s += *(v4f*)&L.rqg[i][4 * lane];
    *(uint2*)&L.qb[2 * lane] = make_uint2(pk(s[0], s[1]), pk(s[2], s[3]));
    const int hb = (lane >> 3) & 1;
    qa = *(v4u*)&L.qb[hb * 64 + 4 * c]; qb = *(v4u*)&L.qb[(1 - hb) * 64 + 4 * c];
  }
  STAMP(4);
  float sc[NG];
  sfor<0, NG>([&](auto G_) {
    constexpr int G = G_, n = (NK - 8 * G) < 8 ? (NK - 8 * G) : 8;
    float t[8];
    sfor<0, n>([&](auto I_) {
      constexpr int i = I_;
      const v4u kk = r[iK + 8 * G + i];
      float a = 0.f, b = 0.f;
      for (int e = 0; e < 4; ++e) { a = dot2(kk[e], qa[e], a); b = dot2(kk[e], qb[e], b); }
      t[i] = a + dpp<XOR8>(b);
    });
    auto fold = [&](auto H_, auto C_, auto N_) {
      constexpr int H = H_, C = C_, m = N_;
      sfor<0, H>([&](auto I_) {
        constexpr int i = I_;
        if constexpr (i + H < m) { const float lo = t[i] + dpp<C>(t[i]), hi = t[i + H] + dpp<C>(t[i + H]); t[i] = (lane & H) ? hi : lo; }
        else if constexpr (i < m) t[i] = t[i] + dpp<C>(t[i]);
      });
    };
    using std::integral_constant;
    fold(integral_constant<int, 4>{}, integral_constant<int, XOR7>{}, integral_constant<int, n>{});
    fold(integral_constant<int, 2>{}, integral_constant<int, XOR2>{}, integral_constant<int, (n < 4 ? n : 4)>{});
    fold(integral_constant<int, 1>{}, integral_constant<int, XOR1>{}, integral_constant<int, (n < 2 ? n : 2)>{});
    const float s = rbf(rbf(t[0]) * 0.08838834764831845f);
    sc[G] = (8 * G + c8 < nvalid) ? s : -INFINITY;
  });
  unsigned pd[NG];
  {
    float m = sc[0];
    for (int G = 1; G < NG; ++G) m = fmaxf(m, sc[G]);
    m = x32max(x16max(max8(m)));
    const int hb = (lane >> 3) & 1;
    if ((lane & 0x37) == 0) L.mx[hb][w] = m;
    wg_barrier();
    const v4f m0 = *(v4f*)&L.mx[hb][0], m1 = *(v4f*)&L.mx[hb][4];
    m = fmaxf(fmaxf(fmaxf(m0[0], m0[1]), fmaxf(m0[2], m0[3])), fmaxf(fmaxf(m1[0], m1[1]), fmaxf(m1[2], m1[3])));
    float l = 0.f;
    for (int G = 0; G < NG; ++G) { const float e = __expf(sc[G] - m); l += e; pd[G] = pk(e, e); }
    l = x32sum(x16sum(sum8(l)));
    if ((lane & 0x37) == 0) L.ls[hb][w] = l;
    wg_barrier();
    if (w == 1) {
      const v4f l0 = *(v4f*)&L.ls[hb][0], l1 = *(v4f*)&L.ls[hb][4];
      l = ((l0[0] + l0[1]) + (l0[2] + l0[3])) + ((l1[0] + l1[1]) + (l1[2] + l1[3]));
      const v4f ml = {m, l, dpp<XOR8>(m), dpp<XOR8>(l)};
      if (lane == 0) st_dev(WS_ML + (unsigned)((g * 16 + j) * 16), tagged(ml, tag), p.ws);
    }
    fence_sched();
  }
  STAMP(5);
  if constexpr (A) {
    v4f a = {0.f, 0.f, 0.f, 0.f};
    sfor<0, 9>([&](auto I_) {
      constexpr int i = I_;
      const unsigned xr = L.xn[gurow0 + 4 * i + g4];
      step(std::integral_constant<int, iGU + i>{});
      a = mac(a, xr, msk, r[iGU + i]);
      fence_sched();
    });
    if (lane < 32) *(v4f*)&L.rg[w][8 * c + 4 * g4] = a;
    wg_barrier();
    if (w == 0) {
      const int mt = lane >> 5, q = lane & 31;
      v4f s = *(v4f*)&L.rg[4 * mt][4 * q];
      for (int i = 1; i < 4; ++i) s += *(v4f*)&L.rg[4 * mt + i][4 * q];
      st_dev(WS_HP + (unsigned)(aidx * 1024 + lane * 16), tagged(s, tag), p.ws);
    }
    fence_sched();
  }
  STAMP(6);
  {
    v4f a = {0.f, 0.f, 0.f, 0.f};
    sfor<0, NK>([&](auto I_) {
      constexpr int i = I_;
      int pb = __builtin_amdgcn_update_dpp(0, (int)pd[i >> 3], 0x150 + (i & 7), 0xf, 0x3, false);
      pb = __builtin_amdgcn_update_dpp(pb, (int)pd[i >> 3], 0x158 + (i & 7), 0xf, 0xc, false);
      step(std::integral_constant<int, iV + i>{});
      a = mac(a, (unsigned)pb, msk, r[iV + i]);
      fence_sched();
    });
    if constexpr (A) { if constexpr (iV + NK - 1 + WIN + 1 < NOPS) sfor<iV + NK + WIN, NOPS>(issue); }
    STAMP(7);
    *(v4f*)&L.ra[w][(g4 >> 1) * 128 + 8 * c + 4 * (g4 & 1)] = a;
    wg_barrier();
    if (w == 0) {
      v4f s = *(v4f*)&L.ra[0][4 * lane];
      for (int i = 1; i < 8; ++i) s += *(v4f*)&L.ra[i][4 * lane];
      st_dev(WS_ACC + (unsigned)((g * 16 + j) * 1024 + lane * 16), tagged(s, tag), p.ws);
    }
    fence_sched();
  }
  STAMP(8);
  if constexpr (A) {
    land<NOPS - iWO>(r[iML + 1]);
    asm volatile("" : "+v"(r[iML]), "+v"(r[iHG]), "+v"(r[iHG + 1]));
    sfor<iWD, iHG>([&](auto N_) { pin(r[N_]); });
    repoll2(r[iHG], r[iHG + 1], ohg0, ohg1, p.ws, tag);
    repoll2(r[iML], r[iML + 1], oml0, oml0 + 128, p.ws, tag);
    const bool diag = (g4 >> 1) == ((lane >> 3) & 1);
    const int rsoff = 8 * c8 + 4 * (g4 & 1);
    {
      unsigned hd[5];
      const v4f v0 = untag(r[iHG]), v1 = untag(r[iHG + 1]);
      const float vx = (r8 & 2) ? ((r8 & 1) ? v1[3] : v1[2]) : ((r8 & 1) ? v1[1] : v1[0]);
      for (int i = 0; i < 5; ++i) {
        float s = i < 4 ? v0[i & 3] : vx; s += dpp<XOR1>(s); s += dpp<XOR2>(s);
        const float o = dpp<XOR7>(s);
        const float gt = rbf((lane & 4) ? o : s), up = rbf((lane & 4) ? s : o);
        const float h = rbf(silu(gt)) * up;
        hd[i] = pk(h, h);
      }
      const unsigned h4 = hd[4] & (r8 < 4 ? 0xffffffffu : 0u);
      v4f a = {0.f, 0.f, 0.f, 0.f};
      for (int i = 0; i < 4; ++i) a = mac(a, hd[i], msk, r[iWD + i]);
      a = mac(a, h4, msk, r[iWD + 4]);
      if (diag) *(v4f*)&L.rs[w][1][g4 >> 1][rsoff] = a;
    }
    float w0, w1;
    {
      const v4f a = untag(r[iML]), b = untag(r[iML + 1]);
      const float ma = w < 4 ? a[0] : a[2], la = w < 4 ? a[1] : a[3], mb = w < 4 ? b[0] : b[2], lb = w < 4 ? b[1] : b[3];
      const float M = max8(fmaxf(ma, mb));
      const float ea = __expf(ma - M), eb = __expf(mb - M);
      const float inv = 1.f / sum8(ea * la + eb * lb);
      w0 = ea * inv; w1 = eb * inv;
    }
    fence_sched();
    STAMP(9);
    const unsigned oac = WS_ACC + (unsigned)((g * 16 + c8) * 1024 + (32 * w + 4 * r8) * 4);
    v4u pa, pb;
    land<1>(r[iWO + 2]);
    ld_dev<0>(pa, oac, p.ws); ld_dev<0>(pb, oac + 8192, p.ws);
    land<0>(pa); land<0>(pb);
    sfor<iWO, NOPS>([&](auto N_) { pin(r[N_]); });
    STAMP(10);
    repoll2(pa, pb, oac, oac + 8192, p.ws, tag);
    STAMP(11);
    {
      const v4f a = untag(pa), b = untag(pb);
      v4f s = {0.f, 0.f, 0.f, 0.f};
      for (int i = 0; i < 4; ++i) { const float o = sum8(w0 * a[i] + w1 * b[i]); s = mac(s, pk(o, o), msk, r[iWO + i]); }
      if (diag) *(v4f*)&L.rs[w][0][g4 >> 1][rsoff] = s;
    }
    wg_barrier();
    if (w == 0) {
      const int bh = lane >> 5, sf = (lane >> 4) & 1;
      v4f s = *(v4f*)&L.rs[0][sf][bh][4 * c];
      for (int i = 1; i < 8; ++i) s += *(v4f*)&L.rs[i][sf][bh][4 * c];
      for (int i = 0; i < 4; ++i) s[i] = x32sum(s[i]);
      if (lane < 32) st_dev(WS_SF + (unsigned)((j * 16 + g) * 512 + lane * 16), tagged(s, tag), p.ws);
    }
    STAMP(12);
  } else {
    const int u = (j - 9) * 16 + g;
    if (u < 36 && w == 1) {
      const int cq = (lane & 3) ^ ((lane & 4) ? 3 : 0);
      const unsigned osf = WS_SF + (unsigned)(((u >> 2) * 16 + (lane >> 2)) * 512 + ((u & 3) * 16 + cq * 4) * 4);
      const int col = u * 16 + cq * 4 + ((lane >> 4) & 1) * 2 + ((lane >> 3) & 1);
      const unsigned xw = L.xraw[col >> 1];
      const float xr = asf((col & 1) ? (xw & 0xffff0000u) : (xw << 16));
      v4u pa, pb;
      ld_dev<0>(pa, osf, p.ws); ld_dev<256>(pb, osf, p.ws);
      land<0>(pa); land<0>(pb);
      repoll2(pa, pb, osf, osf + 256, p.ws, tag);
      STAMP(11);
      const v4f s = untag(pa), f = untag(pb);
      float t[4];
      for (int i = 0; i < 4; ++i) { auto x = __builtin_amdgcn_permlane32_swap(asu(s[i]), asu(f[i]), false, false); t[i] = asf(x[0]) + asf(x[1]); }
      for (int i = 0; i < 2; ++i) { auto x = __builtin_amdgcn_permlane16_swap(asu(t[i]), asu(t[i + 2]), false, false); t[i] = asf(x[0]) + asf(x[1]); }
      const float lo = t[0] + dpp<XOR8>(t[0]), hi = t[1] + dpp<XOR8>(t[1]);
      float y = (lane & 8) ? hi : lo;
      y = rbf(y + dpp<XOR7>(y));
      auto x = __builtin_amdgcn_permlane32_swap(asu(y), asu(y), false, false);
      const float res = xr + rbf(asf(x[0]) * rbf(silu(asf(x[1]))));
      if (lane < 32 && (lane & 4) == 0) *(unsigned short*)(p.out + col * 2) = (unsigned short)(pk(res, res) & 0xffffu);
      if (u == 0 && lane == 0) __hip_atomic_store((unsigned*)(p.ws + WS_EPOCH), epoch + 1, __ATOMIC_RELAXED, __HIP_MEMORY_SCOPE_AGENT);
      STAMP(12);
    }
  }
  if constexpr (A) asm volatile(".LcodeA1:"); else asm volatile(".LcodeB1:");
}

__global__ void __launch_bounds__(512, 2) fused_decode(Params p) {
  __shared__ __attribute__((aligned(16))) Lds L;
  const int lane = threadIdx.x & 63, w = __builtin_amdgcn_readfirstlane(threadIdx.x >> 6);
  const int b = blockIdx.x, s = b >> 3, g = 2 * (b & 7) + (s & 1), j = s >> 1;
  if (j < 9) body<true>(p, L, lane, w, g, j, g * 9 + j);
  else body<false>(p, L, lane, w, g, j, 0);
}

extern "C" void kernel_launch(void* const* d_in, const int* in_sizes, int n_in, void* d_out, int out_size,
                              void* d_ws, size_t ws_size, hipStream_t stream) {
  Params p;
  p.x = (const char*)d_in[0]; p.k = (const char*)d_in[1]; p.v = (const char*)d_in[2]; p.wq = (const char*)d_in[3];
  p.wo = (const char*)d_in[4]; p.wg = (const char*)d_in[6]; p.wu = (const char*)d_in[7]; p.wd = (const char*)d_in[8];
  p.ws = (char*)d_ws; p.out = (char*)d_out;
  fused_decode<<<256, 512, 0, stream>>>(p);
}
```

```cpp
#define STAMPS
#define PROBE 0
#define PSTAT 0
#include <hip/hip_runtime.h>
#include <hip/hip_bf16.h>
#include <stdint.h>
#include <type_traits>

#define NA 14
#define NB 19
#define WIN 8

typedef unsigned v4u __attribute__((ext_vector_type(4)));
typedef float v4f __attribute__((ext_vector_type(4)));
typedef __bf16 bf16x2 __attribute__((ext_vector_type(2)));
typedef __bf16 bf16x8 __attribute__((ext_vector_type(8)));
#define DI __device__ __forceinline__

constexpr int D = 576, SK = 8192, F = 4608;
constexpr int QUADS = SK / 4;
constexpr int QB = QUADS - 72 * NA;
constexpr int NFULL = QB - 56 * (NB - 1);
static_assert(NFULL > 0 && NFULL <= 56, "key split");

constexpr unsigned WS_EPOCH = 0;
constexpr unsigned WS_QP = 4096;
constexpr unsigned WS_HP = WS_QP + 16 * 16 * 512;
constexpr unsigned WS_ML = WS_HP + 144 * 1024;
constexpr unsigned WS_ACC = WS_ML + 4096;
constexpr unsigned WS_SF = WS_ACC + 256 * 1024;
constexpr int SPIN_CAP = 1 << 16;

struct Params {
  const char* x; const char* k; const char* v; const char* wq; const char* wo;
  const char* wg; const char* wu; const char* wd; char* ws; char* out;
};

template <int U, int N, class Fn> DI void sfor(Fn&& f) { if constexpr (U < N) { f(std::integral_constant<int, U>{}); sfor<U + 1, N>(f); } }
DI float asf(unsigned u) { return __builtin_bit_cast(float, u); }
DI unsigned asu(float f) { return __builtin_bit_cast(unsigned, f); }
DI unsigned pk(float lo, float hi) { bf16x2 r = {(__bf16)lo, (__bf16)hi}; return __builtin_bit_cast(unsigned, r); }
DI float rbf(float f) { return asf(pk(f, f) & 0xffff0000u); }
DI float dot2(unsigned a, unsigned b, float c) { return __builtin_amdgcn_fdot2_f32_bf16(__builtin_bit_cast(bf16x2, a), __builtin_bit_cast(bf16x2, b), c, false); }
template <int C> DI float dpp(float x) { return asf(__builtin_amdgcn_mov_dpp((int)asu(x), C, 0xf, 0xf, true)); }
constexpr int XOR1 = 0xB1, XOR2 = 0x4E, XOR7 = 0x141, XOR8 = 0x128;
DI float x16sum(float x) { auto s = __builtin_amdgcn_permlane16_swap(asu(x), asu(x), false, false); return asf(s[0]) + asf(s[1]); }
DI float x32sum(float x) { auto s = __builtin_amdgcn_permlane32_swap(asu(x), asu(x), false, false); return asf(s[0]) + asf(s[1]); }
DI float x16max(float x) { auto s = __builtin_amdgcn_permlane16_swap(asu(x), asu(x), false, false); return fmaxf(asf(s[0]), asf(s[1])); }
DI float x32max(float x) { auto s = __builtin_amdgcn_permlane32_swap(asu(x), asu(x), false, false); return fmaxf(asf(s[0]), asf(s[1])); }
DI float sum8(float x) { x += dpp<XOR1>(x); x += dpp<XOR2>(x); return x + dpp<XOR7>(x); }
DI float max8(float x) { x = fmaxf(x, dpp<XOR1>(x)); x = fmaxf(x, dpp<XOR2>(x)); return fmaxf(x, dpp<XOR7>(x)); }
DI float wavesum(float x) { x = sum8(x); x += dpp<XOR8>(x); return x32sum(x16sum(x)); }
DI float silu(float x) { return x * __builtin_amdgcn_rcpf(1.f + __expf(-x)); }
DI void fence_sched() { __builtin_amdgcn_sched_barrier(0); }
DI void wg_barrier() { asm volatile("s_waitcnt lgkmcnt(0)\n\ts_barrier" ::: "memory"); }

template <int OFF> DI void ld_nt(v4u& r, unsigned voff, const char* base) {
  asm volatile("global_load_dwordx4 %0, %1, %2 offset:%3 nt" : "=v"(r) : "v"(voff), "s"(base), "n"(OFF));
}
template <int OFF> DI void ld_dev(v4u& r, unsigned voff, const char* base) {
  asm volatile("global_load_dwordx4 %0, %1, %2 offset:%3 sc1" : "=v"(r) : "v"(voff), "s"(base), "n"(OFF));
}
DI void st_dev(unsigned voff, v4u d, char* base) {
  asm volatile("global_store_dwordx4 %0, %1, %2 sc1\n\ts_nop 1" :: "v"(voff), "v"(d), "s"(base) : "memory");
}
template <int N> DI void land(v4u& r) { asm volatile("s_waitcnt vmcnt(%1)" : "+v"(r) : "n"(N)); }
DI void pin(v4u& r) { asm volatile("" : "+v"(r)); }
DI v4u tagged(v4f v, unsigned tag) {
  v4u r; for (int i = 0; i < 4; ++i) r[i] = (asu(v[i]) & 0xffffff00u) | tag; return r;
}
DI v4f untag(v4u v) { v4f r; for (int i = 0; i < 4; ++i) r[i] = asf(v[i] & 0xffffff00u); return r; }
DI unsigned tagdiff(v4u v, unsigned tag) { return ((v[0] ^ tag) | (v[1] ^ tag) | (v[2] ^ tag) | (v[3] ^ tag)) & 0xffu; }
DI bool all_ok(unsigned diff) { return __builtin_amdgcn_ballot_w64(diff != 0) == 0; }
DI void repoll1(v4u& a, unsigned oa, const char* ws, unsigned tag) {
  #pragma unroll 1
  for (int s = 0; s < SPIN_CAP && !all_ok(tagdiff(a, tag)); ++s) { ld_dev<0>(a, oa, ws); land<0>(a); }
}
DI void repoll2(v4u& a, v4u& b, unsigned oa, unsigned ob, const char* ws, unsigned tag) {
  #pragma unroll 1
  for (int s = 0; s < SPIN_CAP && !all_ok(tagdiff(a, tag) | tagdiff(b, tag)); ++s) {
    ld_dev<0>(a, oa, ws); ld_dev<0>(b, ob, ws); land<0>(a); land<0>(b);
  }
}
DI v4f mac(v4f acc, unsigned dup, const v4u& msk, const v4u& w) {
  v4u a = {dup & msk[0], dup & msk[1], dup & msk[2], dup & msk[3]};
  return __builtin_amdgcn_mfma_f32_16x16x32_bf16(__builtin_bit_cast(bf16x8, a), __builtin_bit_cast(bf16x8, w), acc, 0, 0, 0);
}

struct Lds {
  unsigned xn[D];
  unsigned xraw[D / 2];
  float rq[8][128];
  float rg[8][128];
  float rqg[8][256];
  unsigned qb[128];
  float mx[2][8];
  float ls[2][8];
  float ra[8][256];
  float rs[8][2][2][64];
};

#define STAMP(i) do { stamp[i] = __builtin_amdgcn_s_memrealtime(); } while (0)

template <bool A> DI void body(const Params& p, Lds& L, const int lane, const int w, const int g, const int j, const int aidx) {
  constexpr int NK = A ? NA : NB;
  constexpr int iX = 2, iWQ = 4, iK = 7, iQG = iK + NK, iGU = iQG + 1, iV = A ? iGU + 9 : iQG + 1;
  constexpr int iWD = iV + NK, iHG = iWD + 5, iML = iHG + 2, iWO = iML + 2, NOPS = A ? iWO + 4 : iV + NK;
  constexpr int NG = (NK + 7) / 8;
  unsigned long long stamp[16] = {};
  STAMP(0);
  const int c = lane & 15, g4 = lane >> 4, r8 = lane >> 3, c8 = lane & 7;
  v4u r[NOPS];

  int quad0, nvalid;
  if (A) { quad0 = (j * 8 + w) * NA; nvalid = NA; }
  else { const int idx = w * 7 + (j - 9); quad0 = 72 * NA + idx * (NB - 1) + (idx < NFULL ? idx : NFULL); nvalid = idx < NFULL ? NB : NB - 1; }
  const unsigned kvrow0 = (unsigned)(g * SK + quad0 * 4) * 256u;
  const unsigned okv = kvrow0 + lane * 16;
  const unsigned okv_last = nvalid == NK ? okv + (NK - 1) * 1024 : kvrow0;
  const int wqrow0 = 72 * (j >> 1) + 9 * w;
  const unsigned owq = (unsigned)(wqrow0 + g4) * 8192u + ((2 * g + (j & 1)) * 128 + 8 * c) * 2;
  const unsigned owq2 = (unsigned)(wqrow0 + 8) * 8192u + ((2 * g + (j & 1)) * 128 + 8 * c) * 2;
  const unsigned oqg = WS_QP + (unsigned)((g * 16 + 2 * w) * 512 + lane * 16);
  const int gurow0 = 144 * (aidx & 3) + 36 * (w & 3);
  const unsigned ogu = (unsigned)(gurow0 + g4) * (F * 2) + ((aidx >> 2) * 128 + 8 * c) * 2;
  const char* wgu = w < 4 ? p.wg : p.wu;
  const unsigned owd = (unsigned)(288 * g + 36 * w + 4 * r8) * (D * 2) + (64 * j + 8 * c8) * 2;
  const unsigned owd4 = (unsigned)(288 * g + 36 * w + 32 + (r8 & 3)) * (D * 2) + (64 * j + 8 * c8) * 2;
  const unsigned owo = (unsigned)(256 * g + 32 * w + 4 * r8) * (D * 2) + (64 * j + 8 * c8) * 2;
  auto hpoff = [&](int col) { return WS_HP + (unsigned)((((col >> 7) * 4 + (lane & 3)) * 2 + ((lane >> 2) & 1)) * 512 + (col & 127) * 4); };
  const unsigned ohg0 = hpoff(288 * g + 36 * w + 4 * r8), ohg1 = hpoff(288 * g + 36 * w + 32);
  const unsigned oml0 = WS_ML + (unsigned)((g * 16 + c8) * 16);

  unsigned junk, epoch;
  auto issue = [&](auto N_) {
    constexpr int n = N_;
    if constexpr (n == 0) {
      unsigned long long pc; unsigned len;
      if constexpr (A) asm volatile("s_getpc_b64 %0\n.LcodeA0:\n\ts_mov_b32 %1, .LcodeA1-.LcodeA0" : "=s"(pc), "=s"(len));
      else asm volatile("s_getpc_b64 %0\n.LcodeB0:\n\ts_mov_b32 %1, .LcodeB1-.LcodeB0" : "=s"(pc), "=s"(len));
      const unsigned o = min((unsigned)(w * 64 + lane) * 128u, len);
      asm volatile("global_load_dword %0, %1, %2" : "=v"(junk) : "v"(o), "s"(pc));
    } else if constexpr (n == 1) {
      asm volatile("global_load_dword %0, %1, %2 sc1" : "=v"(epoch) : "v"(WS_EPOCH), "s"(p.ws));
    } else if constexpr (n == iX) {
      asm volatile("global_load_dwordx4 %0, %1, %2" : "=v"(r[n]) : "v"(lane * 16), "s"(p.x));
    } else if constexpr (n == iX + 1) {
      asm volatile("global_load_dwordx4 %0, %1, %2 offset:1024" : "=v"(r[n]) : "v"(c8 * 16), "s"(p.x));
    } else if constexpr (n < iK) {
      constexpr int i = n - iWQ;
      ld_nt<0>(r[n], i == 2 ? owq2 : owq + i * 4 * 8192, p.wq);
    } else if constexpr (n < iQG || (n >= iV && n < iV + NK)) {
      constexpr bool isk = n < iQG;
      constexpr int i = isk ? n - iK : n - iV;
      const char* base = isk ? p.k : p.v;
      if constexpr (i == NK - 1) ld_nt<0>(r[n], okv_last, base);
      else ld_nt<(i & 3) * 1024>(r[n], okv + (i >> 2) * 4096, base);
    } else if constexpr (n == iQG) {
      ld_dev<0>(r[n], oqg, p.ws);
    } else if constexpr (n < iV) {
      ld_nt<0>(r[n], ogu + (n - iGU) * 4 * (F * 2), wgu);
    } else if constexpr (n < iHG) {
      constexpr int i = n - iWD;
      if constexpr (i == 4) ld_nt<0>(r[n], owd4, p.wd); else ld_nt<i * D * 2>(r[n], owd, p.wd);
    } else if constexpr (n < iML) {
      ld_dev<0>(r[n], n == iHG ? ohg0 : ohg1, p.ws);
    } else if constexpr (n < iWO) {
      ld_dev<(n - iML) * 128>(r[n], oml0, p.ws);
    } else {
      ld_nt<(n - iWO) * D * 2>(r[n], owo, p.wo);
    }
  };
  auto step = [&](auto K_) {
    constexpr int k = K_;
    constexpr int issued = (k + WIN < NOPS) ? k + WIN : NOPS;
    land<issued - 1 - k>(r[k]);
    if constexpr (k + WIN < NOPS) issue(std::integral_constant<int, k + WIN>{});
  };

  sfor<0, iWQ + WIN>(issue);
  v4u msk;
  for (int i = 0; i < 4; ++i) msk[i] = (i == (c8 >> 1)) ? ((lane & 1) ? 0xffff0000u : 0x0000ffffu) : 0u;

  land<WIN>(r[iX + 1]);
  asm volatile("" : "+v"(r[iX]), "+v"(epoch), "+v"(junk));
  STAMP(1);
  const unsigned tag = (epoch + 1) & 0xffu;
  {
    float xv[16];
    for (int i = 0; i < 4; ++i) {
      xv[2 * i] = asf(r[iX][i] << 16); xv[2 * i + 1] = asf(r[iX][i] & 0xffff0000u);
      xv[8 + 2 * i] = asf(r[iX + 1][i] << 16); xv[9 + 2 * i] = asf(r[iX + 1][i] & 0xffff0000u);
    }
    const bool ext = lane < 8;
    float s0 = 0.f, s1 = 0.f;
    for (int i = 0; i < 8; ++i) { s0 += xv[i]; s1 += xv[8 + i]; }
    const float mean = wavesum(s0 + (ext ? s1 : 0.f)) * (1.f / D);
    s0 = 0.f; s1 = 0.f;
    for (int i = 0; i < 8; ++i) { const float a = xv[i] - mean, b = xv[8 + i] - mean; s0 += a * a; s1 += b * b; }
    const float var = rbf(wavesum(s0 + (ext ? s1 : 0.f)) * (1.f / D));
    const float mb = rbf(mean), den = rbf(__builtin_sqrtf(rbf(var + 1e-5f)));
    v4u o[4];
    for (int i = 0; i < 16; ++i) { const float y = rbf(xv[i] - mb) / den; o[i >> 2][i & 3] = pk(y, y); }
    *(v4u*)&L.xn[lane * 8] = o[0]; *(v4u*)&L.xn[lane * 8 + 4] = o[1];
    *(v4u*)&L.xn[512 + c8 * 8] = o[2]; *(v4u*)&L.xn[512 + c8 * 8 + 4] = o[3];
    if (!A) { *(v4u*)&L.xraw[lane * 4] = r[iX]; *(v4u*)&L.xraw[256 + c8 * 4] = r[iX + 1]; }
  }
  {
    const unsigned x0 = L.xn[wqrow0 + g4], x1 = L.xn[wqrow0 + 4 + g4], x2 = g4 == 0 ? L.xn[wqrow0 + 8] : 0u;
    sfor<iWQ, iK>(step);
    v4f a = {0.f, 0.f, 0.f, 0.f};
    a = mac(a, x0, msk, r[iWQ]); a = mac(a, x1, msk, r[iWQ + 1]); a = mac(a, x2, msk, r[iWQ + 2]);
    if (lane < 32) *(v4f*)&L.rq[w][8 * c + 4 * g4] = a;
    wg_barrier();
    if (w == 0 && lane < 32) {
      v4f s = *(v4f*)&L.rq[0][4 * lane];
      for (int i = 1; i < 8; ++i) s += *(v4f*)&L.rq[i][4 * lane];
      st_dev(WS_QP + (unsigned)((g * 16 + j) * 512 + lane * 16), tagged(s, tag), p.ws);
    }
    fence_sched();
  }
  STAMP(2);
  sfor<iK, iQG + 1>(step);
  STAMP(3);
  v4u qa, qb;
  {
    repoll1(r[iQG], oqg, p.ws, tag);
    *(v4f*)&L.rqg[w][4 * lane] = untag(r[iQG]);
    wg_barrier();
    v4f s = *(v4f*)&L.rqg[0][4 * lane];
    for (int i = 1; i < 8; ++i) s += *(v4f*)&L.rqg[i][4 * lane];
    *(uint2*)&L.qb[2 * lane] = make_uint2(pk(s[0], s[1]), pk(s[2], s[3]));
    const int hb = (lane >> 3) & 1;
    qa = *(v4u*)&L.qb[hb * 64 + 4 * c]; qb = *(v4u*)&L.qb[(1 - hb) * 64 + 4 * c];
  }
  STAMP(4);
  float sc[NG];
  sfor<0, NG>([&](auto G_) {
    constexpr int G = G_, n = (NK - 8 * G) < 8 ? (NK - 8 * G) : 8;
    float t[8];
    sfor<0, n>([&](auto I_) {
      constexpr int i = I_;
      const v4u kk = r[iK + 8 * G + i];
      float a = 0.f, b = 0.f;
      for (int e = 0; e < 4; ++e) { a = dot2(kk[e], qa[e], a); b = dot2(kk[e], qb[e], b); }
      t[i] = a + dpp<XOR8>(b);
    });
    auto fold = [&](auto H_, auto C_, auto N_) {
      constexpr int H = H_, C = C_, m = N_;
      sfor<0, H>([&](auto I_) {
        constexpr int i = I_;
        if constexpr (i + H < m) { const float lo = t[i] + dpp<C>(t[i]), hi = t[i + H] + dpp<C>(t[i + H]); t[i] = (lane & H) ? hi : lo; }
        else if constexpr (i < m) t[i] = t[i] + dpp<C>(t[i]);
      });
    };
    using std::integral_constant;
    fold(integral_constant<int, 4>{}, integral_constant<int, XOR7>{}, integral_constant<int, n>{});
    fold(integral_constant<int, 2>{}, integral_constant<int, XOR2>{}, integral_constant<int, (n < 4 ? n : 4)>{});
    fold(integral_constant<int, 1>{}, integral_constant<int, XOR1>{}, integral_constant<int, (n < 2 ? n : 2)>{});
    const float s = rbf(rbf(t[0]) * 0.08838834764831845f);
    sc[G] = (8 * G + c8 < nvalid) ? s : -INFINITY;
  });
  unsigned pd[NG];
  {
    float m = sc[0];
    for (int G = 1; G < NG; ++G) m = fmaxf(m, sc[G]);
    m = x32max(x16max(max8(m)));
    const int hb = (lane >> 3) & 1;
    if ((lane & 0x37) == 0) L.mx[hb][w] = m;
    wg_barrier();
    const v4f m0 = *(v4f*)&L.mx[hb][0], m1 = *(v4f*)&L.mx[hb][4];
    m = fmaxf(fmaxf(fmaxf(m0[0], m0[1]), fmaxf(m0[2], m0[3])), fmaxf(fmaxf(m1[0], m1[1]), fmaxf(m1[2], m1[3])));
    float l = 0.f;
    for (int G = 0; G < NG; ++G) { const float e = __expf(sc[G] - m); l += e; pd[G] = pk(e, e); }
    l = x32sum(x16sum(sum8(l)));
    if ((lane & 0x37) == 0) L.ls[hb][w] = l;
    wg_barrier();
    if (w == 1) {
      const v4f l0 = *(v4f*)&L.ls[hb][0], l1 = *(v4f*)&L.ls[hb][4];
      l = ((l0[0] + l0[1]) + (l0[2] + l0[3])) + ((l1[0] + l1[1]) + (l1[2] + l1[3]));
      const v4f ml = {m, l, dpp<XOR8>(m), dpp<XOR8>(l)};
      if (lane == 0) st_dev(WS_ML + (unsigned)((g * 16 + j) * 16), tagged(ml, tag), p.ws);
    }
    fence_sched();
  }
  STAMP(5);
  if constexpr (A) {
    v4f a = {0.f, 0.f, 0.f, 0.f};
    sfor<0, 9>([&](auto I_) {
      constexpr int i = I_;
      const unsigned xr = L.xn[gurow0 + 4 * i + g4];
      step(std::integral_constant<int, iGU + i>{});
      a = mac(a, xr, msk, r[iGU + i]);
      fence_sched();
    });
    if (lane < 32) *(v4f*)&L.rg[w][8 * c + 4 * g4] = a;
    wg_barrier();
    if (w == 0) {
      const int mt = lane >> 5, q = lane & 31;
      v4f s = *(v4f*)&L.rg[4 * mt][4 * q];
      for (int i = 1; i < 4; ++i) s += *(v4f*)&L.rg[4 * mt + i][4 * q];
      st_dev(WS_HP + (unsigned)(aidx * 1024 + lane * 16), tagged(s, tag), p.ws);
    }
    fence_sched();
  }
  STAMP(6);
  {
    v4f a = {0.f, 0.f, 0.f, 0.f};
    sfor<0, NK>([&](auto I_) {
      constexpr int i = I_;
      int pb = __builtin_amdgcn_update_dpp(0, (int)pd[i >> 3], 0x150 + (i & 7), 0xf, 0x3, false);
      pb = __builtin_amdgcn_update_dpp(pb, (int)pd[i >> 3], 0x158 + (i & 7), 0xf, 0xc, false);
      step(std::integral_constant<int, iV + i>{});
      a = mac(a, (unsigned)pb, msk, r[iV + i]);
      fence_sched();
    });
    if constexpr (A) { if constexpr (iV + NK - 1 + WIN + 1 < NOPS) sfor<iV + NK + WIN, NOPS>(issue); }
    STAMP(7);
    *(v4f*)&L.ra[w][(g4 >> 1) * 128 + 8 * c + 4 * (g4 & 1)] = a;
    wg_barrier();
    if (w == 0) {
      v4f s = *(v4f*)&L.ra[0][4 * lane];
      for (int i = 1; i < 8; ++i) s += *(v4f*)&L.ra[i][4 * lane];
      st_dev(WS_ACC + (unsigned)((g * 16 + j) * 1024 + lane * 16), tagged(s, tag), p.ws);
    }
    fence_sched();
  }
  STAMP(8);
  if constexpr (A) {
    land<NOPS - iWO>(r[iML + 1]);
    asm volatile("" : "+v"(r[iML]), "+v"(r[iHG]), "+v"(r[iHG + 1]));
    sfor<iWD, iHG>([&](auto N_) { pin(r[N_]); });
    repoll2(r[iHG], r[iHG + 1], ohg0, ohg1, p.ws, tag);
    repoll2(r[iML], r[iML + 1], oml0, oml0 + 128, p.ws, tag);
    const bool diag = (g4 >> 1) == ((lane >> 3) & 1);
    const int rsoff = 8 * c8 + 4 * (g4 & 1);
    {
      unsigned hd[5];
      const v4f v0 = untag(r[iHG]), v1 = untag(r[iHG + 1]);
      const float vx = (r8 & 2) ? ((r8 & 1) ? v1[3] : v1[2]) : ((r8 & 1) ? v1[1] : v1[0]);
      for (int i = 0; i < 5; ++i) {
        float s = i < 4 ? v0[i & 3] : vx; s += dpp<XOR1>(s); s += dpp<XOR2>(s);
        const float o = dpp<XOR7>(s);
        const float gt = rbf((lane & 4) ? o : s), up = rbf((lane & 4) ? s : o);
        const float h = rbf(silu(gt)) * up;
        hd[i] = pk(h, h);
      }
      const unsigned h4 = hd[4] & (r8 < 4 ? 0xffffffffu : 0u);
      v4f a = {0.f, 0.f, 0.f, 0.f};
      for (int i = 0; i < 4; ++i) a = mac(a, hd[i], msk, r[iWD + i]);
      a = mac(a, h4, msk, r[iWD + 4]);
      if (diag) *(v4f*)&L.rs[w][1][g4 >> 1][rsoff] = a;
    }
    float w0, w1;
    {
      const v4f a = untag(r[iML]), b = untag(r[iML + 1]);
      const float ma = w < 4 ? a[0] : a[2], la = w < 4 ? a[1] : a[3], mb = w < 4 ? b[0] : b[2], lb = w < 4 ? b[1] : b[3];
      const float M = max8(fmaxf(ma, mb));
      const float ea = __expf(ma - M), eb = __expf(mb - M);
      const float inv = 1.f / sum8(ea * la + eb * lb);
      w0 = ea * inv; w1 = eb * inv;
    }
    fence_sched();
    STAMP(9);
    const unsigned oac = WS_ACC + (unsigned)((g * 16 + c8) * 1024 + (32 * w + 4 * r8) * 4);
    v4u pa, pb;
    land<1>(r[iWO + 2]);
    ld_dev<0>(pa, oac, p.ws); ld_dev<0>(pb, oac + 8192, p.ws);
    land<0>(pa); land<0>(pb);
    sfor<iWO, NOPS>([&](auto N_) { pin(r[N_]); });
    STAMP(10);
    repoll2(pa, pb, oac, oac + 8192, p.ws, tag);
    STAMP(11);
    {
      const v4f a = untag(pa), b = untag(pb);
      v4f s = {0.f, 0.f, 0.f, 0.f};
      for (int i = 0; i < 4; ++i) { const float o = sum8(w0 * a[i] + w1 * b[i]); s = mac(s, pk(o, o), msk, r[iWO + i]); }
      if (diag) *(v4f*)&L.rs[w][0][g4 >> 1][rsoff] = s;
    }
    wg_barrier();
    if (w == 0) {
      const int bh = lane >> 5, sf = (lane >> 4) & 1;
      v4f s = *(v4f*)&L.rs[0][sf][bh][4 * c];
      for (int i = 1; i < 8; ++i) s += *(v4f*)&L.rs[i][sf][bh][4 * c];
      for (int i = 0; i < 4; ++i) s[i] = x32sum(s[i]);
      if (lane < 32) st_dev(WS_SF + (unsigned)((j * 16 + g) * 512 + lane * 16), tagged(s, tag), p.ws);
    }
    STAMP(12);
  } else {
    const int u = (j - 9) * 16 + g;
    if (u < 36 && w == 1) {
      const int cq = (lane & 3) ^ ((lane & 4) ? 3 : 0);
      const unsigned osf = WS_SF + (unsigned)(((u >> 2) * 16 + (lane >> 2)) * 512 + ((u & 3) * 16 + cq * 4) * 4);
      const int col = u * 16 + cq * 4 + ((lane >> 4) & 1) * 2 + ((lane >> 3) & 1);
      const unsigned xw = L.xraw[col >> 1];
      const float xr = asf((col & 1) ? (xw & 0xffff0000u) : (xw << 16));
      v4u pa, pb;
      ld_dev<0>(pa, osf, p.ws); ld_dev<256>(pb, osf, p.ws);
      land<0>(pa); land<0>(pb);
      repoll2(pa, pb, osf, osf + 256, p.ws, tag);
      STAMP(11);
      const v4f s = untag(pa), f = untag(pb);
      float t[4];
      for (int i = 0; i < 4; ++i) { auto x = __builtin_amdgcn_permlane32_swap(asu(s[i]), asu(f[i]), false, false); t[i] = asf(x[0]) + asf(x[1]); }
      for (int i = 0; i < 2; ++i) { auto x = __builtin_amdgcn_permlane16_swap(asu(t[i]), asu(t[i + 2]), false, false); t[i] = asf(x[0]) + asf(x[1]); }
      const float lo = t[0] + dpp<XOR8>(t[0]), hi = t[1] + dpp<XOR8>(t[1]);
      float y = (lane & 8) ? hi : lo;
      y = rbf(y + dpp<XOR7>(y));
      auto x = __builtin_amdgcn_permlane32_swap(asu(y), asu(y), false, false);
      const float res = xr + rbf(asf(x[0]) * rbf(silu(asf(x[1]))));
      if (lane < 32 && (lane & 4) == 0) *(unsigned short*)(p.out + col * 2) = (unsigned short)(pk(res, res) & 0xffffu);
      if (u == 0 && lane == 0) __hip_atomic_store((unsigned*)(p.ws + WS_EPOCH), epoch + 1, __ATOMIC_RELAXED, __HIP_MEMORY_SCOPE_AGENT);
      STAMP(12);
    }
  }
  if constexpr (A) asm volatile(".LcodeA1:"); else asm volatile(".LcodeB1:");
  if (lane == 0 && w == (A ? 0 : 1)) {
    unsigned long long* st = (unsigned long long*)(p.ws + (200u << 20)) + blockIdx.x * 16;
    for (int i = 0; i < 13; ++i) __hip_atomic_store(st + i, stamp[i], __ATOMIC_RELAXED, __HIP_MEMORY_SCOPE_AGENT);
  }
  if (!A && blockIdx.x == 255 && w == 2) {
    while (__builtin_amdgcn_s_memrealtime() - stamp[0] < 4500) __builtin_amdgcn_s_sleep(8);
    unsigned long long* st = (unsigned long long*)(p.ws + (200u << 20));
    unsigned long long t0[4], ti[4];
    for (int i = 0; i < 4; ++i) {
      t0[i] = __hip_atomic_load(st + (lane * 4 + i) * 16, __ATOMIC_RELAXED, __HIP_MEMORY_SCOPE_AGENT);
      ti[i] = __hip_atomic_load(st + (lane * 4 + i) * 16 + PROBE, __ATOMIC_RELAXED, __HIP_MEMORY_SCOPE_AGENT);
    }
    int mn = 1 << 30;
    for (int i = 0; i < 4; ++i) mn = min(mn, (int)(t0[i] - stamp[0]));
    for (int o = 1; o < 64; o <<= 1) mn = min(mn, __shfl_xor(mn, o));
    int mx = 0, sum = 0, cnt = 0, lo = 1 << 30;
    for (int i = 0; i < 4; ++i) { const long long d = (long long)(ti[i] - stamp[0]) - mn; if (d >= 0 && d < 10000) { mx = max(mx, (int)d); lo = min(lo, (int)d); sum += (int)d; ++cnt; } }
    for (int o = 1; o < 64; o <<= 1) { mx = max(mx, __shfl_xor(mx, o)); lo = min(lo, __shfl_xor(lo, o)); sum += __shfl_xor(sum, o); cnt += __shfl_xor(cnt, o); }
    const int val = PSTAT == 0 ? 0 : PSTAT == 1 ? mx : PSTAT == 2 ? sum / max(cnt, 1) : lo;
    const long long until = min(5000 + mn + val, 20000);
    while ((long long)(__builtin_amdgcn_s_memrealtime() - stamp[0]) < until) {}
  }
}

__global__ void __launch_bounds__(512, 2) fused_decode(Params p) {
  __shared__ __attribute__((aligned(16))) Lds L;
  const int lane = threadIdx.x & 63, w = __builtin_amdgcn_readfirstlane(threadIdx.x >> 6);
  const int b = blockIdx.x, s = b >> 3, g = 2 * (b & 7) + (s & 1), j = s >> 1;
  if (j < 9) body<true>(p, L, lane, w, g, j, g * 9 + j);
  else body<false>(p, L, lane, w, g, j, 0);
}

extern "C" void kernel_launch(void* const* d_in, const int* in_sizes, int n_in, void* d_out, int out_size,
                              void* d_ws, size_t ws_size, hipStream_t stream) {
  Params p;
  p.x = (const char*)d_in[0]; p.k = (const char*)d_in[1]; p.v = (const char*)d_in[2]; p.wq = (const char*)d_in[3];
  p.wo = (const char*)d_in[4]; p.wg = (const char*)d_in[6]; p.wu = (const char*)d_in[7]; p.wd = (const char*)d_in[8];
  p.ws = (char*)d_ws; p.out = (char*)d_out;
  fused_decode<<<256, 512, 0, stream>>>(p);
}
```

```cpp
#define STAMPS
#define PROBE 2
#include <hip/hip_runtime.h>
#include <hip/hip_bf16.h>
#include <stdint.h>
#include <type_traits>

#define NA 14
#define NB 19
#define WIN 8

typedef unsigned v4u __attribute__((ext_vector_type(4)));
typedef float v4f __attribute__((ext_vector_type(4)));
typedef __bf16 bf16x2 __attribute__((ext_vector_type(2)));
typedef __bf16 bf16x8 __attribute__((ext_vector_type(8)));
#define DI __device__ __forceinline__

constexpr int D = 576, SK = 8192, F = 4608;
constexpr int QUADS = SK / 4;
constexpr int QB = QUADS - 72 * NA;
constexpr int NFULL = QB - 56 * (NB - 1);
static_assert(NFULL > 0 && NFULL <= 56, "key split");

constexpr unsigned WS_EPOCH = 0;
constexpr unsigned WS_QP = 4096;
constexpr unsigned WS_HP = WS_QP + 16 * 16 * 512;
constexpr unsigned WS_ML = WS_HP + 144 * 1024;
constexpr unsigned WS_ACC = WS_ML + 4096;
constexpr unsigned WS_SF = WS_ACC + 256 * 1024;
constexpr int SPIN_CAP = 1 << 16;

struct Params {
  const char* x; const char* k; const char* v; const char* wq; const char* wo;
  const char* wg; const char* wu; const char* wd; char* ws; char* out;
};

template <int U, int N, class Fn> DI void sfor(Fn&& f) { if constexpr (U < N) { f(std::integral_constant<int, U>{}); sfor<U + 1, N>(f); } }
DI float asf(unsigned u) { return __builtin_bit_cast(float, u); }
DI unsigned asu(float f) { return __builtin_bit_cast(unsigned, f); }
DI unsigned pk(float lo, float hi) { bf16x2 r = {(__bf16)lo, (__bf16)hi}; return __builtin_bit_cast(unsigned, r); }
DI float rbf(float f) { return asf(pk(f, f) & 0xffff0000u); }
DI float dot2(unsigned a, unsigned b, float c) { return __builtin_amdgcn_fdot2_f32_bf16(__builtin_bit_cast(bf16x2, a), __builtin_bit_cast(bf16x2, b), c, false); }
template <int C> DI float dpp(float x) { return asf(__builtin_amdgcn_mov_dpp((int)asu(x), C, 0xf, 0xf, true)); }
constexpr int XOR1 = 0xB1, XOR2 = 0x4E, XOR7 = 0x141, XOR8 = 0x128;
DI float x16sum(float x) { auto s = __builtin_amdgcn_permlane16_swap(asu(x), asu(x), false, false); return asf(s[0]) + asf(s[1]); }
DI float x32sum(float x) { auto s = __builtin_amdgcn_permlane32_swap(asu(x), asu(x), false, false); return asf(s[0]) + asf(s[1]); }
DI float x16max(float x) { auto s = __builtin_amdgcn_permlane16_swap(asu(x), asu(x), false, false); return fmaxf(asf(s[0]), asf(s[1])); }
DI float x32max(float x) { auto s = __builtin_amdgcn_permlane32_swap(asu(x), asu(x), false, false); return fmaxf(asf(s[0]), asf(s[1])); }
DI float sum8(float x) { x += dpp<XOR1>(x); x += dpp<XOR2>(x); return x + dpp<XOR7>(x); }
DI float max8(float x) { x = fmaxf(x, dpp<XOR1>(x)); x = fmaxf(x, dpp<XOR2>(x)); return fmaxf(x, dpp<XOR7>(x)); }
DI float wavesum(float x) { x = sum8(x); x += dpp<XOR8>(x); return x32sum(x16sum(x)); }
DI float silu(float x) { return x * __builtin_amdgcn_rcpf(1.f + __expf(-x)); }
DI void fence_sched() { __builtin_amdgcn_sched_barrier(0); }
DI void wg_barrier() { asm volatile("s_waitcnt lgkmcnt(0)\n\ts_barrier" ::: "memory"); }

template <int OFF> DI void ld_nt(v4u& r, unsigned voff, const char* base) {
  asm volatile("global_load_dwordx4 %0, %1, %2 offset:%3 nt" : "=v"(r) : "v"(voff), "s"(base), "n"(OFF));
}
template <int OFF> DI void ld_dev(v4u& r, unsigned voff, const char* base) {
  asm volatile("global_load_dwordx4 %0, %1, %2 offset:%3 sc1" : "=v"(r) : "v"(voff), "s"(base), "n"(OFF));
}
DI void st_dev(unsigned voff, v4u d, char* base) {
  asm volatile("global_store_dwordx4 %0, %1, %2 sc1\n\ts_nop 1" :: "v"(voff), "v"(d), "s"(base) : "memory");
}
template <int N> DI void land(v4u& r) { asm volatile("s_waitcnt vmcnt(%1)" : "+v"(r) : "n"(N)); }
DI void pin(v4u& r) { asm volatile("" : "+v"(r)); }
DI v4u tagged(v4f v, unsigned tag) {
  v4u r; for (int i = 0; i < 4; ++i) r[i] = (asu(v[i]) & 0xffffff00u) | tag; return r;
}
DI v4f untag(v4u v) { v4f r; for (int i = 0; i < 4; ++i) r[i] = asf(v[i] & 0xffffff00u); return r; }
DI unsigned tagdiff(v4u v, unsigned tag) { return ((v[0] ^ tag) | (v[1] ^ tag) | (v[2] ^ tag) | (v[3] ^ tag)) & 0xffu; }
DI bool all_ok(unsigned diff) { return __builtin_amdgcn_ballot_w64(diff != 0) == 0; }
DI void repoll1(v4u& a, unsigned oa, const char* ws, unsigned tag) {
  #pragma unroll 1
  for (int s = 0; s < SPIN_CAP && !all_ok(tagdiff(a, tag)); ++s) { ld_dev<0>(a, oa, ws); land<0>(a); }
}
DI void repoll2(v4u& a, v4u& b, unsigned oa, unsigned ob, const char* ws, unsigned tag) {
  #pragma unroll 1
  for (int s = 0; s < SPIN_CAP && !all_ok(tagdiff(a, tag) | tagdiff(b, tag)); ++s) {
    ld_dev<0>(a, oa, ws); ld_dev<0>(b, ob, ws); land<0>(a); land<0>(b);
  }
}
DI v4f mac(v4f acc, unsigned dup, const v4u& msk, const v4u& w) {
  v4u a = {dup & msk[0], dup & msk[1], dup & msk[2], dup & msk[3]};
  return __builtin_amdgcn_mfma_f32_16x16x32_bf16(__builtin_bit_cast(bf16x8, a), __builtin_bit_cast(bf16x8, w), acc, 0, 0, 0);
}

struct Lds {
  unsigned xn[D];
  unsigned xraw[D / 2];
  float rq[8][128];
  float rg[8][128];
  float rqg[8][256];
  unsigned qb[128];
  float mx[2][8];
  float ls[2][8];
  float ra[8][256];
  float rs[8][2][2][64];
};

#define STAMP(i) do { stamp[i] = __builtin_amdgcn_s_memrealtime(); } while (0)

template <bool A> DI void body(const Params& p, Lds& L, const int lane, const int w, const int g, const int j, const int aidx) {
  constexpr int NK = A ? NA : NB;
  constexpr int iX = 2, iWQ = 4, iK = 7, iQG = iK + NK, iGU = iQG + 1, iV = A ? iGU + 9 : iQG + 1;
  constexpr int iWD = iV + NK, iHG = iWD + 5, iML = iHG + 2, iWO = iML + 2, NOPS = A ? iWO + 4 : iV + NK;
  constexpr int NG = (NK + 7) / 8;
  unsigned long long stamp[16] = {};
  STAMP(0);
  const int c = lane & 15, g4 = lane >> 4, r8 = lane >> 3, c8 = lane & 7;
  v4u r[NOPS];

  int quad0, nvalid;
  if (A) { quad0 = (j * 8 + w) * NA; nvalid = NA; }
  else { const int idx = w * 7 + (j - 9); quad0 = 72 * NA + idx * (NB - 1) + (idx < NFULL ? idx : NFULL); nvalid = idx < NFULL ? NB : NB - 1; }
  const unsigned kvrow0 = (unsigned)(g * SK + quad0 * 4) * 256u;
  const unsigned okv = kvrow0 + lane * 16;
  const unsigned okv_last = nvalid == NK ? okv + (NK - 1) * 1024 : kvrow0;
  const int wqrow0 = 72 * (j >> 1) + 9 * w;
  const unsigned owq = (unsigned)(wqrow0 + g4) * 8192u + ((2 * g + (j & 1)) * 128 + 8 * c) * 2;
  const unsigned owq2 = (unsigned)(wqrow0 + 8) * 8192u + ((2 * g + (j & 1)) * 128 + 8 * c) * 2;
  const unsigned oqg = WS_QP + (unsigned)((g * 16 + 2 * w) * 512 + lane * 16);
  const int gurow0 = 144 * (aidx & 3) + 36 * (w & 3);
  const unsigned ogu = (unsigned)(gurow0 + g4) * (F * 2) + ((aidx >> 2) * 128 + 8 * c) * 2;
  const char* wgu = w < 4 ? p.wg : p.wu;
  const unsigned owd = (unsigned)(288 * g + 36 * w + 4 * r8) * (D * 2) + (64 * j + 8 * c8) * 2;
  const unsigned owd4 = (unsigned)(288 * g + 36 * w + 32 + (r8 & 3)) * (D * 2) + (64 * j + 8 * c8) * 2;
  const unsigned owo = (unsigned)(256 * g + 32 * w + 4 * r8) * (D * 2) + (64 * j + 8 * c8) * 2;
  auto hpoff = [&](int col) { return WS_HP + (unsigned)((((col >> 7) * 4 + (lane & 3)) * 2 + ((lane >> 2) & 1)) * 512 + (col & 127) * 4); };
  const unsigned ohg0 = hpoff(288 * g + 36 * w + 4 * r8), ohg1 = hpoff(288 * g + 36 * w + 32);
  const unsigned oml0 = WS_ML + (unsigned)((g * 16 + c8) * 16);

  unsigned junk, epoch;
  auto issue = [&](auto N_) {
    constexpr int n = N_;
    if constexpr (n == 0) {
      unsigned long long pc; unsigned len;
      if constexpr (A) asm volatile("s_getpc_b64 %0\n.LcodeA0:\n\ts_mov_b32 %1, .LcodeA1-.LcodeA0" : "=s"(pc), "=s"(len));
      else asm volatile("s_getpc_b64 %0\n.LcodeB0:\n\ts_mov_b32 %1, .LcodeB1-.LcodeB0" : "=s"(pc), "=s"(len));
      const unsigned o = min((unsigned)(w * 64 + lane) * 128u, len);
      asm volatile("global_load_dword %0, %1, %2" : "=v"(junk) : "v"(o), "s"(pc));
    } else if constexpr (n == 1) {
      asm volatile("global_load_dword %0, %1, %2 sc1" : "=v"(epoch) : "v"(WS_EPOCH), "s"(p.ws));
    } else if constexpr (n == iX) {
      asm volatile("global_load_dwordx4 %0, %1, %2" : "=v"(r[n]) : "v"(lane * 16), "s"(p.x));
    } else if constexpr (n == iX + 1) {
      asm volatile("global_load_dwordx4 %0, %1, %2 offset:1024" : "=v"(r[n]) : "v"(c8 * 16), "s"(p.x));
    } else if constexpr (n < iK) {
      constexpr int i = n - iWQ;
      ld_nt<0>(r[n], i == 2 ? owq2 : owq + i * 4 * 8192, p.wq);
    } else if constexpr (n < iQG || (n >= iV && n < iV + NK)) {
      constexpr bool isk = n < iQG;
      constexpr int i = isk ? n - iK : n - iV;
      const char* base = isk ? p.k : p.v;
      if constexpr (i == NK - 1) ld_nt<0>(r[n], okv_last, base);
      else ld_nt<(i & 3) * 1024>(r[n], okv + (i >> 2) * 4096, base);
    } else if constexpr (n == iQG) {
      ld_dev<0>(r[n], oqg, p.ws);
    } else if constexpr (n < iV) {
      ld_nt<0>(r[n], ogu + (n - iGU) * 4 * (F * 2), wgu);
    } else if constexpr (n < iHG) {
      constexpr int i = n - iWD;
      if constexpr (i == 4) ld_nt<0>(r[n], owd4, p.wd); else ld_nt<i * D * 2>(r[n], owd, p.wd);
    } else if constexpr (n < iML) {
      ld_dev<0>(r[n], n == iHG ? ohg0 : ohg1, p.ws);
    } else if constexpr (n < iWO) {
      ld_dev<(n - iML) * 128>(r[n], oml0, p.ws);
    } else {
      ld_nt<(n - iWO) * D * 2>(r[n], owo, p.wo);
    }
  };
  auto step = [&](auto K_) {
    constexpr int k = K_;
    constexpr int issued = (k + WIN < NOPS) ? k + WIN : NOPS;
    land<issued - 1 - k>(r[k]);
    if constexpr (k + WIN < NOPS) issue(std::integral_constant<int, k + WIN>{});
  };

  sfor<0, iWQ + WIN>(issue);
  v4u msk;
  for (int i = 0; i < 4; ++i) msk[i] = (i == (c8 >> 1)) ? ((lane & 1) ? 0xffff0000u : 0x0000ffffu) : 0u;

  land<WIN>(r[iX + 1]);
  asm volatile("" : "+v"(r[iX]), "+v"(epoch), "+v"(junk));
  STAMP(1);
  const unsigned tag = (epoch + 1) & 0xffu;
  {
    float xv[16];
    for (int i = 0; i < 4; ++i) {
      xv[2 * i] = asf(r[iX][i] << 16); xv[2 * i + 1] = asf(r[iX][i] & 0xffff0000u);
      xv[8 + 2 * i] = asf(r[iX + 1][i] << 16); xv[9 + 2 * i] = asf(r[iX + 1][i] & 0xffff0000u);
    }
    const bool ext = lane < 8;
    float s0 = 0.f, s1 = 0.f;
    for (int i = 0; i < 8; ++i) { s0 += xv[i]; s1 += xv[8 + i]; }
    const float mean = wavesum(s0 + (ext ? s1 : 0.f)) * (1.f / D);
    s0 = 0.f; s1 = 0.f;
    for (int i = 0; i < 8; ++i) { const float a = xv[i] - mean, b = xv[8 + i] - mean; s0 += a * a; s1 += b * b; }
    const float var = rbf(wavesum(s0 + (ext ? s1 : 0.f)) * (1.f / D));
    const float mb = rbf(mean), den = rbf(__builtin_sqrtf(rbf(var + 1e-5f)));
    v4u o[4];
    for (int i = 0; i < 16; ++i) { const float y = rbf(xv[i] - mb) / den; o[i >> 2][i & 3] = pk(y, y); }
    *(v4u*)&L.xn[lane * 8] = o[0]; *(v4u*)&L.xn[lane * 8 + 4] = o[1];
    *(v4u*)&L.xn[512 + c8 * 8] = o[2]; *(v4u*)&L.xn[512 + c8 * 8 + 4] = o[3];
    if (!A) { *(v4u*)&L.xraw[lane * 4] = r[iX]; *(v4u*)&L.xraw[256 + c8 * 4] = r[iX + 1]; }
  }
  {
    const unsigned x0 = L.xn[wqrow0 + g4], x1 = L.xn[wqrow0 + 4 + g4], x2 = g4 == 0 ? L.xn[wqrow0 + 8] : 0u;
    sfor<iWQ, iK>(step);
    v4f a = {0.f, 0.f, 0.f, 0.f};
    a = mac(a, x0, msk, r[iWQ]); a = mac(a, x1, msk, r[iWQ + 1]); a = mac(a, x2, msk, r[iWQ + 2]);
    if (lane < 32) *(v4f*)&L.rq[w][8 * c + 4 * g4] = a;
    wg_barrier();
    if (w == 0 && lane < 32) {
      v4f s = *(v4f*)&L.rq[0][4 * lane];
      for (int i = 1; i < 8; ++i) s += *(v4f*)&L.rq[i][4 * lane];
      st_dev(WS_QP + (unsigned)((g * 16 + j) * 512 + lane * 16), tagged(s, tag), p.ws);
    }
    fence_sched();
  }
  STAMP(2);
  sfor<iK, iQG + 1>(step);
  STAMP(3);
  v4u qa, qb;
  {
    repoll1(r[iQG], oqg, p.ws, tag);
    *(v4f*)&L.rqg[w][4 * lane] = untag(r[iQG]);
    wg_barrier();
    v4f s = *(v4f*)&L.rqg[0][4 * lane];
    for (int i = 1; i < 8; ++i) s += *(v4f*)&L.rqg[i][4 * lane];
    *(uint2*)&L.qb[2 * lane] = make_uint2(pk(s[0], s[1]), pk(s[2], s[3]));
    const int hb = (lane >> 3) & 1;
    qa = *(v4u*)&L.qb[hb * 64 + 4 * c]; qb = *(v4u*)&L.qb[(1 - hb) * 64 + 4 * c];
  }
  STAMP(4);
  float sc[NG];
  sfor<0, NG>([&](auto G_) {
    constexpr int G = G_, n = (NK - 8 * G) < 8 ? (NK - 8 * G) : 8;
    float t[8];
    sfor<0, n>([&](auto I_) {
      constexpr int i = I_;
      const v4u kk = r[iK + 8 * G + i];
      float a = 0.f, b = 0.f;
      for (int e = 0; e < 4; ++e) { a = dot2(kk[e], qa[e], a); b = dot2(kk[e], qb[e], b); }
      t[i] = a + dpp<XOR8>(b);
    });
    auto fold = [&](auto H_, auto C_, auto N_) {
      constexpr int H = H_, C = C_, m = N_;
      sfor<0, H>([&](auto I_) {
        constexpr int i = I_;
        if constexpr (i + H < m) { const float lo = t[i] + dpp<C>(t[i]), hi = t[i + H] + dpp<C>(t[i + H]); t[i] = (lane & H) ? hi : lo; }
        else if constexpr (i < m) t[i] = t[i] + dpp<C>(t[i]);
      });
    };
    using std::integral_constant;
    fold(integral_constant<int, 4>{}, integral_constant<int, XOR7>{}, integral_constant<int, n>{});
    fold(integral_constant<int, 2>{}, integral_constant<int, XOR2>{}, integral_constant<int, (n < 4 ? n : 4)>{});
    fold(integral_constant<int, 1>{}, integral_constant<int, XOR1>{}, integral_constant<int, (n < 2 ? n : 2)>{});
    const float s = rbf(rbf(t[0]) * 0.08838834764831845f);
    sc[G] = (8 * G + c8 < nvalid) ? s : -INFINITY;
  });
  unsigned pd[NG];
  {
    float m = sc[0];
    for (int G = 1; G < NG; ++G) m = fmaxf(m, sc[G]);
    m = x32max(x16max(max8(m)));
    const int hb = (lane >> 3) & 1;
    if ((lane & 0x37) == 0) L.mx[hb][w] = m;
    wg_barrier();
    const v4f m0 = *(v4f*)&L.mx[hb][0], m1 = *(v4f*)&L.mx[hb][4];
    m = fmaxf(fmaxf(fmaxf(m0[0], m0[1]), fmaxf(m0[2], m0[3])), fmaxf(fmaxf(m1[0], m1[1]), fmaxf(m1[2], m1[3])));
    float l = 0.f;
    for (int G = 0; G < NG; ++G) { const float e = __expf(sc[G] - m); l += e; pd[G] = pk(e, e); }
    l = x32sum(x16sum(sum8(l)));
    if ((lane & 0x37) == 0) L.ls[hb][w] = l;
    wg_barrier();
    if (w == 1) {
      const v4f l0 = *(v4f*)&L.ls[hb][0], l1 = *(v4f*)&L.ls[hb][4];
      l = ((l0[0] + l0[1]) + (l0[2] + l0[3])) + ((l1[0] + l1[1]) + (l1[2] + l1[3]));
      const v4f ml = {m, l, dpp<XOR8>(m), dpp<XOR8>(l)};
      if (lane == 0) st_dev(WS_ML + (unsigned)((g * 16 + j) * 16), tagged(ml, tag), p.ws);
    }
    fence_sched();
  }
  STAMP(5);
  if constexpr (A) {
    v4f a = {0.f, 0.f, 0.f, 0.f};
    sfor<0, 9>([&](auto I_) {
      constexpr int i = I_;
      const unsigned xr = L.xn[gurow0 + 4 * i + g4];
      step(std::integral_constant<int, iGU + i>{});
      a = mac(a, xr, msk, r[iGU + i]);
      fence_sched();
    });
    if (lane < 32) *(v4f*)&L.rg[w][8 * c + 4 * g4] = a;
    wg_barrier();
    if (w == 0) {
      const int mt = lane >> 5, q = lane & 31;
      v4f s = *(v4f*)&L.rg[4 * mt][4 * q];
      for (int i = 1; i < 4; ++i) s += *(v4f*)&L.rg[4 * mt + i][4 * q];
      st_dev(WS_HP + (unsigned)(aidx * 1024 + lane * 16), tagged(s, tag), p.ws);
    }
    fence_sched();
  }
  STAMP(6);
  {
    v4f a = {0.f, 0.f, 0.f, 0.f};
    sfor<0, NK>([&](auto I_) {
      constexpr int i = I_;
      int pb = __builtin_amdgcn_update_dpp(0, (int)pd[i >> 3], 0x150 + (i & 7), 0xf, 0x3, false);
      pb = __builtin_amdgcn_update_dpp(pb, (int)pd[i >> 3], 0x158 + (i & 7), 0xf, 0xc, false);
      step(std::integral_constant<int, iV + i>{});
      a = mac(a, (unsigned)pb, msk, r[iV + i]);
      fence_sched();
    });
    if constexpr (A) { if constexpr (iV + NK - 1 + WIN + 1 < NOPS) sfor<iV + NK + WIN, NOPS>(issue); }
    STAMP(7);
    *(v4f*)&L.ra[w][(g4 >> 1) * 128 + 8 * c + 4 * (g4 & 1)] = a;
    wg_barrier();
    if (w == 0) {
      v4f s = *(v4f*)&L.ra[0][4 * lane];
      for (int i = 1; i < 8; ++i) s += *(v4f*)&L.ra[i][4 * lane];
      st_dev(WS_ACC + (unsigned)((g * 16 + j) * 1024 + lane * 16), tagged(s, tag), p.ws);
    }
    fence_sched();
  }
  STAMP(8);
  if constexpr (A) {
    land<NOPS - iWO>(r[iML + 1]);
    asm volatile("" : "+v"(r[iML]), "+v"(r[iHG]), "+v"(r[iHG + 1]));
    sfor<iWD, iHG>([&](auto N_) { pin(r[N_]); });
    repoll2(r[iHG], r[iHG + 1], ohg0, ohg1, p.ws, tag);
    repoll2(r[iML], r[iML + 1], oml0, oml0 + 128, p.ws, tag);
    const bool diag = (g4 >> 1) == ((lane >> 3) & 1);
    const int rsoff = 8 * c8 + 4 * (g4 & 1);
    {
      unsigned hd[5];
      const v4f v0 = untag(r[iHG]), v1 = untag(r[iHG + 1]);
      const float vx = (r8 & 2) ? ((r8 & 1) ? v1[3] : v1[2]) : ((r8 & 1) ? v1[1] : v1[0]);
      for (int i = 0; i < 5; ++i) {
        float s = i < 4 ? v0[i & 3] : vx; s += dpp<XOR1>(s); s += dpp<XOR2>(s);
        const float o = dpp<XOR7>(s);
        const float gt = rbf((lane & 4) ? o : s), up = rbf((lane & 4) ? s : o);
        const float h = rbf(silu(gt)) * up;
        hd[i] = pk(h, h);
      }
      const unsigned h4 = hd[4] & (r8 < 4 ? 0xffffffffu : 0u);
      v4f a = {0.f, 0.f, 0.f, 0.f};
      for (int i = 0; i < 4; ++i) a = mac(a, hd[i], msk, r[iWD + i]);
      a = mac(a, h4, msk, r[iWD + 4]);
      if (diag) *(v4f*)&L.rs[w][1][g4 >> 1][rsoff] = a;
    }
    float w0, w1;
    {
      const v4f a = untag(r[iML]), b = untag(r[iML + 1]);
      const float ma = w < 4 ? a[0] : a[2], la = w < 4 ? a[1] : a[3], mb = w < 4 ? b[0] : b[2], lb = w < 4 ? b[1] : b[3];
      const float M = max8(fmaxf(ma, mb));
      const float ea = __expf(ma - M), eb = __expf(mb - M);
      const float inv = 1.f / sum8(ea * la + eb * lb);
      w0 = ea * inv; w1 = eb * inv;
    }
    fence_sched();
    STAMP(9);
    const unsigned oac = WS_ACC + (unsigned)((g * 16 + c8) * 1024 + (32 * w + 4 * r8) * 4);
    v4u pa, pb;
    land<1>(r[iWO + 2]);
    ld_dev<0>(pa, oac, p.ws); ld_dev<0>(pb, oac + 8192, p.ws);
    land<0>(pa); land<0>(pb);
    sfor<iWO, NOPS>([&](auto N_) { pin(r[N_]); });
    STAMP(10);
    repoll2(pa, pb, oac, oac + 8192, p.ws, tag);
    STAMP(11);
    {
      const v4f a = untag(pa), b = untag(pb);
      v4f s = {0.f, 0.f, 0.f, 0.f};
      for (int i = 0; i < 4; ++i) { const float o = sum8(w0 * a[i] + w1 * b[i]); s = mac(s, pk(o, o), msk, r[iWO + i]); }
      if (diag) *(v4f*)&L.rs[w][0][g4 >> 1][rsoff] = s;
    }
    wg_barrier();
    if (w == 0) {
      const int bh = lane >> 5, sf = (lane >> 4) & 1;
      v4f s = *(v4f*)&L.rs[0][sf][bh][4 * c];
      for (int i = 1; i < 8; ++i) s += *(v4f*)&L.rs[i][sf][bh][4 * c];
      for (int i = 0; i < 4; ++i) s[i] = x32sum(s[i]);
      if (lane < 32) st_dev(WS_SF + (unsigned)((j * 16 + g) * 512 + lane * 16), tagged(s, tag), p.ws);
    }
    STAMP(12);
  } else {
    const int u = (j - 9) * 16 + g;
    if (u < 36 && w == 1) {
      const int cq = (lane & 3) ^ ((lane & 4) ? 3 : 0);
      const unsigned osf = WS_SF + (unsigned)(((u >> 2) * 16 + (lane >> 2)) * 512 + ((u & 3) * 16 + cq * 4) * 4);
      const int col = u * 16 + cq * 4 + ((lane >> 4) & 1) * 2 + ((lane >> 3) & 1);
      const unsigned xw = L.xraw[col >> 1];
      const float xr = asf((col & 1) ? (xw & 0xffff0000u) : (xw << 16));
      v4u pa, pb;
      ld_dev<0>(pa, osf, p.ws); ld_dev<256>(pb, osf, p.ws);
      land<0>(pa); land<0>(pb);
      repoll2(pa, pb, osf, osf + 256, p.ws, tag);
      STAMP(11);
      const v4f s = untag(pa), f = untag(pb);
      float t[4];
      for (int i = 0; i < 4; ++i) { auto x = __builtin_amdgcn_permlane32_swap(asu(s[i]), asu(f[i]), false, false); t[i] = asf(x[0]) + asf(x[1]); }
      for (int i = 0; i < 2; ++i) { auto x = __builtin_amdgcn_permlane16_swap(asu(t[i]), asu(t[i + 2]), false, false); t[i] = asf(x[0]) + asf(x[1]); }
      const float lo = t[0] + dpp<XOR8>(t[0]), hi = t[1] + dpp<XOR8>(t[1]);
      float y = (lane & 8) ? hi : lo;
      y = rbf(y + dpp<XOR7>(y));
      auto x = __builtin_amdgcn_permlane32_swap(asu(y), asu(y), false, false);
      const float res = xr + rbf(asf(x[0]) * rbf(silu(asf(x[1]))));
      if (lane < 32 && (lane & 4) == 0) *(unsigned short*)(p.out + col * 2) = (unsigned short)(pk(res, res) & 0xffffu);
      if (u == 0 && lane == 0) __hip_atomic_store((unsigned*)(p.ws + WS_EPOCH), epoch + 1, __ATOMIC_RELAXED, __HIP_MEMORY_SCOPE_AGENT);
      STAMP(12);
    }
  }
  if constexpr (A) asm volatile(".LcodeA1:"); else asm volatile(".LcodeB1:");
  if (lane == 0 && w == (A ? 0 : 1)) {
    unsigned long long* st = (unsigned long long*)(p.ws + (200u << 20)) + blockIdx.x * 16;
    for (int i = 0; i < 13; ++i) __hip_atomic_store(st + i, stamp[i], __ATOMIC_RELAXED, __HIP_MEMORY_SCOPE_AGENT);
  }
  if (!A && blockIdx.x == 255 && w == 2) {
    unsigned long long* st = (unsigned long long*)(p.ws + (201u << 20));
    unsigned long long t0 = stamp[0];
    if (PROBE == 1 || PROBE == 2) { const unsigned long long t = __hip_atomic_load(st, __ATOMIC_RELAXED, __HIP_MEMORY_SCOPE_AGENT); if (t0 - t < 4000) t0 = t; }
    if (PROBE == 3) { if (lane == 0) __hip_atomic_store(st, t0, __ATOMIC_RELAXED, __HIP_MEMORY_SCOPE_AGENT); }
    else while (__builtin_amdgcn_s_memrealtime() - t0 < 5000) {}
  }
}

__global__ void __launch_bounds__(512, 2) fused_decode(Params p) {
  __shared__ __attribute__((aligned(16))) Lds L;
  const int lane = threadIdx.x & 63, w = __builtin_amdgcn_readfirstlane(threadIdx.x >> 6);
  const int b = blockIdx.x, s = b >> 3, g = 2 * (b & 7) + (s & 1), j = s >> 1;
  if (j < 9) body<true>(p, L, lane, w, g, j, g * 9 + j);
  else body<false>(p, L, lane, w, g, j, 0);
}

__global__ void __launch_bounds__(512, 2) tiny(char* ws) {
  const unsigned long long t = __builtin_amdgcn_s_memrealtime();
  unsigned long long* st = (unsigned long long*)(ws + (201u << 20));
  __shared__ unsigned big[9216];
  big[threadIdx.x * 18] = (unsigned)t; __syncthreads();
  asm volatile("v_mov_b32 v151, 0" ::: "v151");
  if (big[(threadIdx.x * 18 + 18) % 9216] == 12345u) st[1] = 1;
  if (PROBE == 3) {
    unsigned long long t0 = __hip_atomic_load(st, __ATOMIC_RELAXED, __HIP_MEMORY_SCOPE_AGENT);
    if (t - t0 > 5000) t0 = t;
    while (__builtin_amdgcn_s_memrealtime() - t0 < 5000) {}
  } else if (blockIdx.x == 0 && threadIdx.x == 0) __hip_atomic_store(st, t, __ATOMIC_RELAXED, __HIP_MEMORY_SCOPE_AGENT);
}

extern "C" void kernel_launch(void* const* d_in, const int* in_sizes, int n_in, void* d_out, int out_size,
                              void* d_ws, size_t ws_size, hipStream_t stream) {
  Params p;
  p.x = (const char*)d_in[0]; p.k = (const char*)d_in[1]; p.v = (const char*)d_in[2]; p.wq = (const char*)d_in[3];
  p.wo = (const char*)d_in[4]; p.wg = (const char*)d_in[6]; p.wu = (const char*)d_in[7]; p.wd = (const char*)d_in[8];
  p.ws = (char*)d_ws; p.out = (char*)d_out;
  if (PROBE == 1 || PROBE == 5) tiny<<<1, 64, 0, stream>>>(p.ws);
  if (PROBE == 2) tiny<<<256, 512, 0, stream>>>(p.ws);
  if (PROBE == 4) (void)hipMemsetAsync(p.ws + (202u << 20), 0, 256, stream);
  fused_decode<<<256, 512, 0, stream>>>(p);
  if (PROBE == 3) tiny<<<1, 64, 0, stream>>>(p.ws);
}
```

```cpp
#include <hip/hip_runtime.h>
#include <hip/hip_bf16.h>
#include <stdint.h>
#include <type_traits>

#define CA 112
#define WIN 5
#define XA 2
#define XB 2
#define QPOS 10
#define HGP 2
#define R1 1
#define R2 4
#define PW 0
#define S1 1
#define SL1 0
#define S2 1
#define SL2 0

typedef unsigned v4u __attribute__((ext_vector_type(4)));
typedef float v4f __attribute__((ext_vector_type(4)));
typedef __bf16 bf16x2 __attribute__((ext_vector_type(2)));
typedef __bf16 bf16x8 __attribute__((ext_vector_type(8)));
#define DI __device__ __forceinline__

constexpr int D = 576, SK = 8192, F = 4608;
constexpr int QUADS = SK / 4;
constexpr int NA = (CA + 7) / 8;
constexpr int QB = QUADS - 9 * CA, BLO = QB / 7, BEX = QB - 7 * BLO;
constexpr int NB = (BLO + (BEX ? 1 : 0) + 7) / 8;
static_assert(BLO >= 8 * (NB - 1), "key split");

constexpr unsigned WS_EPOCH = 0;
constexpr unsigned WS_QP = 4096;
constexpr unsigned WS_HP = WS_QP + 16 * 16 * 512;
constexpr unsigned WS_ML = WS_HP + 144 * 1024;
constexpr unsigned WS_ACC = WS_ML + 4096;
constexpr unsigned ACC_STEP = 256 * 1024;
constexpr unsigned WS_SF = WS_ACC + R1 * ACC_STEP;
constexpr unsigned SF_STEP = 65536, SF_UNIT = 28 * 64;
static_assert(R2 % 4 == 0, "copies are written four at a time");
constexpr int SPIN_CAP = 1 << 16;

struct Params {
  const char* x; const char* k; const char* v; const char* wq; const char* wo;
  const char* wg; const char* wu; const char* wd; char* ws; char* out;
};

template <int U, int N, class Fn> DI void sfor(Fn&& f) { if constexpr (U < N) { f(std::integral_constant<int, U>{}); sfor<U + 1, N>(f); } }
DI float asf(unsigned u) { return __builtin_bit_cast(float, u); }
DI unsigned asu(float f) { return __builtin_bit_cast(unsigned, f); }
DI unsigned pk(float lo, float hi) { bf16x2 r = {(__bf16)lo, (__bf16)hi}; return __builtin_bit_cast(unsigned, r); }
DI float rbf(float f) { return asf(pk(0.f, f)); }
DI float dot2(unsigned a, unsigned b, float c) { return __builtin_amdgcn_fdot2_f32_bf16(__builtin_bit_cast(bf16x2, a), __builtin_bit_cast(bf16x2, b), c, false); }
template <int C> DI float dpp(float x) { return asf(__builtin_amdgcn_mov_dpp((int)asu(x), C, 0xf, 0xf, true)); }
constexpr int XOR1 = 0xB1, XOR2 = 0x4E, XOR7 = 0x141, XOR8 = 0x128;
DI float x16sum(float x) { auto s = __builtin_amdgcn_permlane16_swap(asu(x), asu(x), false, false); return asf(s[0]) + asf(s[1]); }
DI float x32sum(float x) { auto s = __builtin_amdgcn_permlane32_swap(asu(x), asu(x), false, false); return asf(s[0]) + asf(s[1]); }
DI float x16max(float x) { auto s = __builtin_amdgcn_permlane16_swap(asu(x), asu(x), false, false); return fmaxf(asf(s[0]), asf(s[1])); }
DI float x32max(float x) { auto s = __builtin_amdgcn_permlane32_swap(asu(x), asu(x), false, false); return fmaxf(asf(s[0]), asf(s[1])); }
DI float sum8(float x) { x += dpp<XOR1>(x); x += dpp<XOR2>(x); return x + dpp<XOR7>(x); }
DI float max8(float x) { x = fmaxf(x, dpp<XOR1>(x)); x = fmaxf(x, dpp<XOR2>(x)); return fmaxf(x, dpp<XOR7>(x)); }
DI float wavesum(float x) { x = sum8(x); x += dpp<XOR8>(x); return x32sum(x16sum(x)); }
DI float silu(float x) { return x * __builtin_amdgcn_rcpf(1.f + __expf(-x)); }
DI void fence_sched() { __builtin_amdgcn_sched_barrier(0); }
DI void wg_barrier() { asm volatile("s_waitcnt lgkmcnt(0)\n\ts_barrier" ::: "memory"); }

template <int OFF> DI void ld_nt(v4u& r, unsigned voff, const char* base) {
  asm volatile("global_load_dwordx4 %0, %1, %2 offset:%3 nt" : "=v"(r) : "v"(voff), "s"(base), "n"(OFF));
}
template <int OFF> DI void ld_dev(v4u& r, unsigned voff, const char* base) {
  asm volatile("global_load_dwordx4 %0, %1, %2 offset:%3 sc1" : "=v"(r) : "v"(voff), "s"(base), "n"(OFF));
}
DI void st_dev(unsigned voff, v4u d, char* base) {
  asm volatile("global_store_dwordx4 %0, %1, %2 sc1\n\ts_nop 1" :: "v"(voff), "v"(d), "s"(base) : "memory");
}
template <int N> DI void land(v4u& r) { asm volatile("s_waitcnt vmcnt(%1)" : "+v"(r) : "n"(N)); }
template <int N> DI void wait_vm() { asm volatile("s_waitcnt vmcnt(%0)" :: "n"(N)); }
DI void pinf(v4f& r) { asm volatile("" : "+v"(r)); }
DI void pin(v4u& r) { asm volatile("" : "+v"(r)); }
DI v4u tagged(v4f v, unsigned tag) {
  v4u r; for (int i = 0; i < 4; ++i) r[i] = (asu(v[i]) & 0xffffff00u) | tag; return r;
}
DI v4f untag(v4u v) { v4f r; for (int i = 0; i < 4; ++i) r[i] = asf(v[i] & 0xffffff00u); return r; }
DI unsigned tagdiff(v4u v, unsigned tag) { return ((v[0] ^ tag) | (v[1] ^ tag) | (v[2] ^ tag) | (v[3] ^ tag)) & 0xffu; }
DI bool all_ok(unsigned diff) { return __builtin_amdgcn_ballot_w64(diff != 0) == 0; }
DI void repoll1(v4u& a, unsigned oa, const char* ws, unsigned tag) {
  #pragma unroll 1
  for (int s = 0; s < SPIN_CAP && !all_ok(tagdiff(a, tag)); ++s) { ld_dev<0>(a, oa, ws); land<0>(a); }
}
DI void repoll2(v4u& a, v4u& b, unsigned oa, unsigned ob, const char* ws, unsigned tag) {
  #pragma unroll 1
  for (int s = 0; s < SPIN_CAP && !all_ok(tagdiff(a, tag) | tagdiff(b, tag)); ++s) {
    ld_dev<0>(a, oa, ws); ld_dev<0>(b, ob, ws); land<0>(a); land<0>(b);
  }
}
struct Poll { v4u a0, b0, a1, b1, a2, b2; };
#define POLL_SEND(A0, A3, B0, B3) \
  "global_load_dwordx4 v[" #A0 ":" #A3 "], %[oa], %[ws] sc1\n\tglobal_load_dwordx4 v[" #B0 ":" #B3 "], %[ob], %[ws] sc1\n\t" \
  "s_sub_u32 %[k], %[k], 1\n\ts_cselect_b32 %[k], 0, %[k]\n\ts_cselect_b32 %[st], 0, %[st]\n\t" \
  "v_add_u32 %[oa], %[st], %[oa]\n\tv_add_u32 %[ob], %[st], %[ob]\n\ts_sleep %[sl]\n\t"
#define POLL_TEST(A0, A1, A2, A3, B0, B1, B2, B3) \
  "s_waitcnt vmcnt(%[w])\n\t" \
  "v_bitop3_b32 %[t0], v" #A0 ", v" #A1 ", %[tag] bitop3:0x7e\n\tv_bitop3_b32 %[t1], v" #A2 ", v" #A3 ", %[tag] bitop3:0x7e\n\t" \
  "v_bitop3_b32 %[t2], v" #B0 ", v" #B1 ", %[tag] bitop3:0x7e\n\tv_or3_b32 %[t0], %[t0], %[t1], %[t2]\n\t" \
  "v_bitop3_b32 %[t1], v" #B2 ", v" #B3 ", %[tag] bitop3:0x7e\n\tv_bitop3_b32 %[t0], %[t0], %[t1], %[ff] bitop3:0xa8\n\t" \
  "v_cmp_ne_u32 vcc, 0, %[t0]\n\t" \
  "v_mov_b64 v[156:157], v[" #A0 ":" #A1 "]\n\tv_mov_b64 v[158:159], v[" #A2 ":" #A3 "]\n\t" \
  "v_mov_b64 v[160:161], v[" #B0 ":" #B1 "]\n\tv_mov_b64 v[162:163], v[" #B2 ":" #B3 "]\n\t" \
  "s_cbranch_vccz .Lpoll_done%=\n\t"
#define POLL_S0 POLL_SEND(132, 135, 136, 139)
#define POLL_S1 POLL_SEND(140, 143, 144, 147)
#define POLL_S2 POLL_SEND(148, 151, 152, 155)
#define POLL_T0 POLL_TEST(132, 133, 134, 135, 136, 137, 138, 139)
#define POLL_T1 POLL_TEST(140, 141, 142, 143, 144, 145, 146, 147)
#define POLL_T2 POLL_TEST(148, 149, 150, 151, 152, 153, 154, 155)
#define POLL_ASM(PRE, LOOP) \
  asm volatile(PRE ".Lpoll_loop%=:\n\t" LOOP "s_sub_u32 %[n], %[n], 1\n\ts_cbranch_scc0 .Lpoll_loop%=\n.Lpoll_done%=:" \
    : "=&{v[156:159]}"(a), "=&{v[160:163]}"(b), "=&{v[132:135]}"(P.a0), "=&{v[136:139]}"(P.b0), "=&{v[140:143]}"(P.a1), "=&{v[144:147]}"(P.b1), \
      "=&{v[148:151]}"(P.a2), "=&{v[152:155]}"(P.b2), [oa] "+v"(oa), [ob] "+v"(ob), [k] "+s"(k), [st] "+s"(step), [n] "+s"(n), \
      [t0] "=&v"(t0), [t1] "=&v"(t1), [t2] "=&v"(t2) \
    : [ws] "s"(ws), [tag] "v"(tag), [ff] "s"(0xffu), [w] "n"(2 * (S - 1)), [sl] "n"(SL) : "vcc", "scc")
template <int S, int R, int SL> DI void poll2(v4u& a, v4u& b, Poll& P, unsigned oa, unsigned ob, unsigned step, const char* ws, unsigned tag) {
  unsigned k = R - 1, n = SPIN_CAP, t0, t1, t2;
  if constexpr (S == 1) POLL_ASM(POLL_S0, POLL_T0 POLL_S0);
  if constexpr (S == 2) POLL_ASM(POLL_S0 POLL_S1, POLL_T0 POLL_S0 POLL_T1 POLL_S1);
  if constexpr (S == 3) POLL_ASM(POLL_S0 POLL_S1 POLL_S2, POLL_T0 POLL_S0 POLL_T1 POLL_S1 POLL_T2 POLL_S2);
}
DI void drain(Poll& P) {
  asm volatile("s_waitcnt vmcnt(0)" : "+{v[132:135]}"(P.a0), "+{v[136:139]}"(P.b0), "+{v[140:143]}"(P.a1), "+{v[144:147]}"(P.b1),
               "+{v[148:151]}"(P.a2), "+{v[152:155]}"(P.b2));
}
template <int N, int M> DI void wait_vm_w0(int w) {
  asm volatile("s_cmp_lg_u32 %0, 0\n\ts_cbranch_scc1 .Lw_other%= ; @IFNOTW0\n\ts_waitcnt vmcnt(%2)\n\ts_branch .Lw_done%=\n"
               ".Lw_other%=:\n\ts_waitcnt vmcnt(%1)\n.Lw_done%=:" :: "s"(w), "n"(N), "n"(N + M) : "scc");
}
DI v4f mac(v4f acc, unsigned dup, const v4u& msk, const v4u& w) {
  v4u a = {dup & msk[0], dup & msk[1], dup & msk[2], dup & msk[3]};
  return __builtin_amdgcn_mfma_f32_16x16x32_bf16(__builtin_bit_cast(bf16x8, a), __builtin_bit_cast(bf16x8, w), acc, 0, 0, 0);
}

struct Lds {
  unsigned short x[D];
  float rq[8][128];
  float rg[8][128];
  float rqg[8][256];
  unsigned qb[128];
  float mx[2][8];
  float ls[2][8];
  float ra[8][256];
  float rs[8][2][64];
};

#define STAMP(i) do {} while (0)

template <bool A> struct Ops {
  static constexpr int NK = A ? NA : NB, NG = (NK + 7) / 8;
  static constexpr int iK = A ? 9 : 0;
  static constexpr int iQG = QPOS;
  static constexpr int iKend = iK + NK + 1;
  static constexpr int iV = iKend, XP = A ? NK : HGP, NX = A ? 1 : 2, iX = iV + XP;
  static constexpr int iT = iV + NK + NX, NT = A ? 4 : 6, N = iT + NT;
  static constexpr int kidx(int i) { return iK + i + (iK + i >= iQG ? 1 : 0); }
  static constexpr int vidx(int i) { return iV + i + (i >= XP ? NX : 0); }
  static constexpr int X = A ? XA : XB;
  static constexpr int head(int k) {
    int h = k + WIN;
    if (k >= iKend && h < iKend + WIN + X) h = iKend + WIN + X;
    return h < N ? h : N;
  }
};

constexpr unsigned KVSTEP = 8192;
struct KvMap { unsigned o, last; int nvalid; };
DI KvMap kvmap(const bool A, const int lane, const int w, const int g, const int j) {
  const int jb = j - 9, nk = A ? NA : NB, cnt = A ? CA : BLO + (jb < BEX ? 1 : 0);
  const int quad0 = (A ? j * CA : 9 * CA + jb * BLO + (jb < BEX ? jb : BEX)) + w;
  const int nvalid = w < cnt - 8 * (nk - 1) ? nk : nk - 1;
  const unsigned row0 = (unsigned)(g * SK + quad0 * 4) * 256u, o = row0 + lane * 16;
  return {o, nvalid == nk ? o + (nk - 1) * KVSTEP : row0, nvalid};
}

struct Pro {
  v4u msk; unsigned epoch; float mb, rden;
};

template <bool A> DI void body(const Params& p, Lds& L, Pro& pro, v4u (&r0)[WIN], const unsigned o0,
                               const int lane, const int w, const int g, const int j) {
  using O = Ops<A>;
  constexpr int NK = O::NK, NG = O::NG, iK = O::iK, iQG = O::iQG, iKend = O::iKend, iV = O::iV;
  constexpr int iX = O::iX, NX = O::NX, iT = O::iT, NOPS = O::N;
  const int c = lane & 15, g4 = lane >> 4, r8 = lane >> 3, c8 = lane & 7, aidx = g * 9 + j;
  const v4u msk = pro.msk;
  const unsigned tag = (pro.epoch + 1) & 0xffu;
  v4u r[NOPS];
  sfor<0, WIN>([&](auto N_) { r[N_] = r0[N_]; });

  const KvMap km = kvmap(A, lane, w, g, j);
  const unsigned okv = km.o, okv_last = km.last;
  const int nvalid = km.nvalid;
  const unsigned oqg = WS_QP + (unsigned)((g * 16 + 2 * w) * 512 + lane * 16);
  const int gurow0 = 144 * (aidx & 3) + 36 * (w & 3);
  const char* wgu = w < 4 ? p.wg : p.wu;
  const int bidx = g * 7 + (j - 9), part = bidx / 9, cb = A ? j : bidx - 9 * part;
  const bool tail_on = A || bidx < 108;
  const int trow = A ? 256 * g + 32 * w : (tail_on ? 384 * part + 48 * w : 0);
  const unsigned ot0 = tail_on ? (unsigned)(trow + 4 * r8) * (D * 2) + (64 * cb + 8 * c8) * 2 : 0u;
  const unsigned ot1 = tail_on ? (unsigned)(trow + 32 + 2 * r8) * (D * 2) + (64 * cb + 8 * c8) * 2 : 0u;
  auto hpoff = [&](int col) { return WS_HP + (unsigned)((((col >> 7) * 4 + (lane & 3)) * 2 + ((lane >> 2) & 1)) * 512 + (col & 127) * 4); };
  const unsigned ox0 = A ? WS_ML + (unsigned)((g * 16 + c) * 16) : hpoff(trow + 4 * r8), ox1 = hpoff(trow + 32 + 4 * (r8 >> 1));

  auto issue = [&](auto N_) {
    constexpr int n = N_;
    if constexpr (n < iK) {
      ld_nt<0>(r[n], o0 + n * 4 * (F * 2), wgu);
    } else if constexpr (n == iQG) {
      ld_dev<0>(r[n], oqg, p.ws);
    } else if constexpr (n >= iX && n < iX + NX) {
      ld_dev<0>(r[n], n == iX ? ox0 : ox1, p.ws);
    } else if constexpr (n < iT) {
      constexpr bool isk = n < iKend;
      constexpr int i = isk ? n - iK - (n > iQG ? 1 : 0) : n - iV - (n > iX ? NX : 0);
      const char* base = isk ? p.k : p.v;
      if constexpr (i == NK - 1) ld_nt<0>(r[n], okv_last, base);
      else ld_nt<0>(r[n], okv, base + i * KVSTEP);
    } else {
      constexpr int i = n - iT;
      if constexpr (i < 4) ld_nt<i * D * 2>(r[n], ot0, A ? p.wo : p.wd); else ld_nt<(i - 4) * D * 2>(r[n], ot1, p.wd);
    }
  };
  auto step = [&](auto K_) {
    constexpr int k = K_;
    if constexpr (k < WIN) wait_vm<O::head(k) - 1 - k>(); else land<O::head(k) - 1 - k>(r[k]);
    if constexpr (k + 1 == iKend) { if constexpr (O::head(k) < NOPS) issue(std::integral_constant<int, O::head(k)>{}); }
    else sfor<O::head(k), O::head(k + 1)>(issue);
  };

  if constexpr (A) {
    unsigned xr[9];
    for (int i = 0; i < 9; ++i) { const float y = rbf(asf((unsigned)L.x[gurow0 + 4 * i + g4] << 16) - pro.mb) * pro.rden; xr[i] = pk(y, y); }
    v4f a = {0.f, 0.f, 0.f, 0.f};
    sfor<0, 9>([&](auto I_) {
      constexpr int i = I_;
      step(I_);
      pinf(a);
      a = mac(a, xr[i], msk, r[i]);
      fence_sched();
    });
    if (lane < 32) *(v4f*)&L.rg[w][8 * c + 4 * g4] = a;
    wg_barrier();
    if (w == 2) {
      const int mt = lane >> 5, q = lane & 31;
      v4f s = *(v4f*)&L.rg[4 * mt][4 * q];
      for (int i = 1; i < 4; ++i) s += *(v4f*)&L.rg[4 * mt + i][4 * q];
      st_dev(WS_HP + (unsigned)(aidx * 1024 + lane * 16), tagged(s, tag), p.ws);
    }
    fence_sched();
  }
  STAMP(6);
  sfor<iK, iQG + 1>(step);
  STAMP(3);
  v4u qa, qb;
  {
    repoll1(r[iQG], oqg, p.ws, tag);
    *(v4f*)&L.rqg[w][4 * lane] = untag(r[iQG]);
    wg_barrier();
    v4f s = *(v4f*)&L.rqg[0][4 * lane];
    for (int i = 1; i < 8; ++i) s += *(v4f*)&L.rqg[i][4 * lane];
    *(uint2*)&L.qb[2 * lane] = make_uint2(pk(s[0], s[1]), pk(s[2], s[3]));
    const int hb = (lane >> 3) & 1;
    qa = *(v4u*)&L.qb[hb * 64 + 4 * c]; qb = *(v4u*)&L.qb[(1 - hb) * 64 + 4 * c];
    fence_sched();
  }
  STAMP(4);
  float sc[NG], t[8];
  auto score = [&](auto Q_) {
    constexpr int q = Q_, G = q >> 3, i = q & 7, n = (NK - 8 * G) < 8 ? (NK - 8 * G) : 8;
    const v4u kk = r[O::kidx(q)];
    float a = 0.f, b = 0.f;
    for (int e = 0; e < 4; ++e) { a = dot2(kk[e], qa[e], a); b = dot2(kk[e], qb[e], b); }
    t[i] = a + dpp<XOR8>(b);
    if constexpr (i == n - 1) {
      auto fold = [&](auto H_, auto C_, auto N_) {
        constexpr int H = H_, C = C_, m = N_;
        sfor<0, H>([&](auto I_) {
          constexpr int i = I_;
          if constexpr (i + H < m) { const float lo = t[i] + dpp<C>(t[i]), hi = t[i + H] + dpp<C>(t[i + H]); t[i] = (lane & H) ? hi : lo; }
          else if constexpr (i < m) t[i] = t[i] + dpp<C>(t[i]);
        });
      };
      using std::integral_constant;
      fold(integral_constant<int, 4>{}, integral_constant<int, XOR7>{}, integral_constant<int, n>{});
      fold(integral_constant<int, 2>{}, integral_constant<int, XOR2>{}, integral_constant<int, (n < 4 ? n : 4)>{});
      fold(integral_constant<int, 1>{}, integral_constant<int, XOR1>{}, integral_constant<int, (n < 2 ? n : 2)>{});
      const float s = rbf(rbf(t[0]) * 0.08838834764831845f);
      sc[G] = (8 * G + c8 < nvalid) ? s : -INFINITY;
    }
  };
  constexpr int i0 = iQG - iK;
  static_assert(3 * (NK - i0) >= NK, "score backlog");
  sfor<i0, NK>([&](auto I_) {
    constexpr int i = I_, lo = i == i0 ? 0 : (i < 3 * (i - i0) ? i : 3 * (i - i0)), hi = i + 1 < 3 * (i - i0 + 1) ? i + 1 : 3 * (i - i0 + 1);
    step(std::integral_constant<int, O::kidx(i)>{});
    sfor<lo, hi>(score);
    fence_sched();
  });
  STAMP(14);
  constexpr int x0 = O::head(iKend - 1) + 1 < NOPS ? O::head(iKend - 1) + 1 : NOPS, x1 = O::head(iKend), xm = (x0 + x1 + 1) / 2;
  unsigned pd[NG];
  {
    float m = sc[0];
    for (int G = 1; G < NG; ++G) m = fmaxf(m, sc[G]);
    m = x32max(x16max(max8(m)));
    const int hb = (lane >> 3) & 1;
    if ((lane & 0x37) == 0) L.mx[hb][w] = m;
    wg_barrier();
    sfor<x0, xm>(issue);
    const v4f m0 = *(v4f*)&L.mx[hb][0], m1 = *(v4f*)&L.mx[hb][4];
    m = fmaxf(fmaxf(fmaxf(m0[0], m0[1]), fmaxf(m0[2], m0[3])), fmaxf(fmaxf(m1[0], m1[1]), fmaxf(m1[2], m1[3])));
    float l = 0.f;
    for (int G = 0; G < NG; ++G) { const float e = __expf(sc[G] - m); l += e; pd[G] = pk(e, e); }
    l = x32sum(x16sum(sum8(l)));
    if ((lane & 0x37) == 0) L.ls[hb][w] = l;
    wg_barrier();
    sfor<xm, x1>(issue);
    if (w == 1) {
      const v4f l0 = *(v4f*)&L.ls[hb][0], l1 = *(v4f*)&L.ls[hb][4];
      l = ((l0[0] + l0[1]) + (l0[2] + l0[3])) + ((l1[0] + l1[1]) + (l1[2] + l1[3]));
      const v4f ml = {m, l, dpp<XOR8>(m), dpp<XOR8>(l)};
      if (lane == 0) st_dev(WS_ML + (unsigned)((g * 16 + j) * 16), tagged(ml, tag), p.ws);
    }
    fence_sched();
  }
  STAMP(5);
  float w0 = 0.f, w1 = 0.f;
  unsigned hd[6];
  auto extras = [&]() {
    if constexpr (A) {
      pin(r[iX]);
      repoll1(r[iX], ox0, p.ws, tag);
      const v4f a = untag(r[iX]);
      const float m = w < 4 ? a[0] : a[2], l = w < 4 ? a[1] : a[3];
      float M = max8(m); M = fmaxf(M, dpp<XOR8>(M));
      const float e = __expf(m - M);
      float t = sum8(e * l); t += dpp<XOR8>(t);
      w0 = e * __builtin_amdgcn_rcpf(t); w1 = dpp<XOR8>(w0);
    } else {
      sfor<iX, iX + NX>(step);
      pin(r[iX]);
      repoll2(r[iX], r[iX + 1], ox0, ox1, p.ws, tag);
      const v4f v = untag(r[iX]), z = untag(r[iX + 1]);
      const bool b0 = lane & 1, b1 = lane & 2, b2 = lane & 4;
      const float x0 = (lane & 8) ? z[2] : z[0], x1 = (lane & 8) ? z[3] : z[1];
      const float a0 = (b0 ? v[1] : v[0]) + dpp<XOR1>(b0 ? v[0] : v[1]), a1 = (b0 ? v[3] : v[2]) + dpp<XOR1>(b0 ? v[2] : v[3]);
      const float s4 = (b1 ? a1 : a0) + dpp<XOR2>(b1 ? a0 : a1);
      float s2 = (b0 ? x1 : x0) + dpp<XOR1>(b0 ? x0 : x1); s2 += dpp<XOR2>(s2);
      const float o = dpp<0x1B>(dpp<XOR7>(b2 ? s4 : s2));
      const float h = rbf(silu(rbf(b2 ? o : s4))) * rbf(b2 ? s2 : o);
      const unsigned hh = pk(h, h);
      sfor<0, 6>([&](auto I_) {
        constexpr int i = I_;
        const int t = __builtin_amdgcn_update_dpp(0, (int)hh, 0x150 + i, 0xf, 0x3, false);
        hd[i] = (unsigned)__builtin_amdgcn_update_dpp(t, (int)hh, 0x158 + i, 0xf, 0xc, false);
      });
    }
    fence_sched();
    STAMP(13);
  };
  {
    v4f a = {0.f, 0.f, 0.f, 0.f};
    sfor<0, NK>([&](auto I_) {
      constexpr int i = I_;
      if constexpr (i == O::XP) extras();
      int pb = __builtin_amdgcn_update_dpp(0, (int)pd[i >> 3], 0x150 + (i & 7), 0xf, 0x3, false);
      pb = __builtin_amdgcn_update_dpp(pb, (int)pd[i >> 3], 0x158 + (i & 7), 0xf, 0xc, false);
      step(std::integral_constant<int, O::vidx(i)>{});
      if constexpr (i == NK - 1) sfor<O::head(O::vidx(i) + 1), NOPS>(issue);
      a = mac(a, (unsigned)pb, msk, r[O::vidx(i)]);
      fence_sched();
    });
    STAMP(7);
    *(v4f*)&L.ra[w][(g4 >> 1) * 128 + 8 * c + 4 * (g4 & 1)] = a;
    wg_barrier();
    constexpr int left = A ? 4 : 0;
    if (w == 0) {
      v4f s = *(v4f*)&L.ra[0][4 * lane];
      for (int i = 1; i < 8; ++i) s += *(v4f*)&L.ra[i][4 * lane];
      const v4u d = tagged(s, tag);
      for (int i = 0; i < R1; ++i) st_dev(WS_ACC + i * ACC_STEP + (unsigned)((g * 16 + j) * 1024 + lane * 16), d, p.ws);
      asm volatile("; @W0");
    }
    STAMP(8);
    wait_vm_w0<left, R1>(w);
    fence_sched();
  }
  if constexpr (A) {
    extras();
    if constexpr (PW < 4) wait_vm<PW>();
  }
  STAMP(9);
  Poll P1;
  v4f s = {0.f, 0.f, 0.f, 0.f};
  if constexpr (A) {
    const unsigned oac = WS_ACC + (unsigned)(g * 16 * 1024 + (32 * w + 4 * r8) * 4);
    v4u pa, pb;
    poll2<S1, R1, SL1>(pa, pb, P1, oac + c * 1024, oac + (c ^ 8) * 1024, ACC_STEP, p.ws, tag);
    sfor<iT, NOPS>([&](auto N_) { pin(r[N_]); });
    STAMP(10);
    const v4f a = untag(pa), b = untag(pb);
    for (int i = 0; i < 4; ++i) { const float o = sum8(w0 * a[i] + w1 * b[i]); s = mac(s, pk(o, o), msk, r[iT + i]); }
  } else {
    sfor<iT, NOPS>([&](auto N_) { pin(r[N_]); });
    for (int i = 0; i < 6; ++i) s = mac(s, hd[i], msk, r[iT + i]);
  }
  if ((g4 >> 1) == ((lane >> 3) & 1)) *(v4f*)&L.rs[w][g4 >> 1][8 * c8 + 4 * (g4 & 1)] = s;
  wg_barrier();
  if (w == (A ? 0 : 2) && tail_on) {
    const int bh = lane >> 5, hf = (lane >> 4) & 1;
    v4f t = *(v4f*)&L.rs[4 * hf][bh][4 * c];
    for (int i = 1; i < 4; ++i) t += *(v4f*)&L.rs[4 * hf + i][bh][4 * c];
    for (int i = 0; i < 4; ++i) t[i] = x32sum(x16sum(t[i]));
    const v4u d = tagged(t, tag);
    const unsigned o = WS_SF + (unsigned)(g4 * SF_STEP + (4 * cb + (c >> 2)) * SF_UNIT + (A ? g : 16 + part) * 64 + (c & 3) * 16);
    for (int i = 0; i < R2 / 4; ++i) st_dev(o + i * 4 * SF_STEP, d, p.ws);
  }
  STAMP(11);
  if constexpr (A) {
    drain(P1);
  } else {
    const int u = (j - 9) * 16 + g;
    if (u < 36 && w == 1) {
      const int cq = (lane & 3) ^ ((lane & 4) ? 3 : 0), pr = lane >> 2;
      const unsigned osf = WS_SF + (unsigned)(u * SF_UNIT + cq * 16);
      const int col = u * 16 + cq * 4 + ((lane >> 4) & 1) * 2 + ((lane >> 3) & 1);
      const float xr = asf((unsigned)L.x[col] << 16);
      Poll P2;
      v4u pa, pb;
      poll2<S2, R2, SL2>(pa, pb, P2, osf + pr * 64, osf + (16 + (pr < 11 ? pr : 11)) * 64, SF_STEP, p.ws, tag);
      STAMP(10);
      const v4f s = untag(pa);
      v4f f = untag(pb);
      float t[4];
      for (int i = 0; i < 4; ++i) { auto x = __builtin_amdgcn_permlane32_swap(asu(s[i]), pr < 12 ? asu(f[i]) : 0u, false, false); t[i] = asf(x[0]) + asf(x[1]); }
      for (int i = 0; i < 2; ++i) { auto x = __builtin_amdgcn_permlane16_swap(asu(t[i]), asu(t[i + 2]), false, false); t[i] = asf(x[0]) + asf(x[1]); }
      const float lo = t[0] + dpp<XOR8>(t[0]), hi = t[1] + dpp<XOR8>(t[1]);
      float y = (lane & 8) ? hi : lo;
      y = rbf(y + dpp<XOR7>(y));
      auto x = __builtin_amdgcn_permlane32_swap(asu(y), asu(y), false, false);
      const float res = xr + rbf(asf(x[0]) * rbf(silu(asf(x[1]))));
      if (lane < 32 && (lane & 4) == 0) *(unsigned short*)(p.out + col * 2) = (unsigned short)(pk(res, res) & 0xffffu);
      if (u == 0 && lane == 0) __hip_atomic_store((unsigned*)(p.ws + WS_EPOCH), pro.epoch + 1, __ATOMIC_RELAXED, __HIP_MEMORY_SCOPE_AGENT);
      STAMP(12);
      drain(P2);
    }
  }
  if constexpr (A) asm volatile(".LcodeA1:"); else asm volatile(".LcodeB1:");
}

__global__ void __launch_bounds__(512, 2) fused_decode(Params p) {
  __shared__ __attribute__((aligned(16))) Lds L;
  Pro pro;
  STAMP(0);
  const int lane = threadIdx.x & 63, w = __builtin_amdgcn_readfirstlane(threadIdx.x >> 6);
  const int b = blockIdx.x, s = b >> 3, g = 2 * (b & 7) + (s & 1), j = s >> 1;
  const bool A = j < 9;
  const int c = lane & 15, g4 = lane >> 4, c8 = lane & 7;
  v4u xa; unsigned xt, junk0;
  asm volatile("global_load_dwordx4 %0, %1, %2" : "=v"(xa) : "v"(lane * 16), "s"(p.x));
  asm volatile("global_load_ushort %0, %1, %2 offset:1024" : "=v"(xt) : "v"(lane * 2), "s"(p.x));
  asm volatile("global_load_dword %0, %1, %2 sc1" : "=v"(pro.epoch) : "v"(WS_EPOCH), "s"(p.ws));
  {
    unsigned long long pc; unsigned la, lb;
    asm volatile("s_getpc_b64 %0\n.Lcode0:\n\ts_mov_b32 %1, .LcodeA1-.Lcode0\n\ts_mov_b32 %2, .LcodeB1-.Lcode0" : "=s"(pc), "=s"(la), "=s"(lb));
    const unsigned o = min((unsigned)(lane * 8 + w) * 128u, max(la, lb));
    asm volatile("global_load_dword %0, %1, %2" : "=v"(junk0) : "v"(o), "s"(pc));
  }
  asm volatile("s_barrier");
  const int wqrow0 = 72 * (j >> 1) + 9 * w;
  const unsigned owq = (unsigned)(wqrow0 + g4) * 8192u + ((2 * g + (j & 1)) * 128 + 8 * c) * 2;
  const unsigned owq2 = (unsigned)(wqrow0 + 8) * 8192u + ((2 * g + (j & 1)) * 128 + 8 * c) * 2;
  v4u wq0, wq1, wq2;
  ld_nt<0>(wq0, owq, p.wq); ld_nt<0>(wq1, owq + 4 * 8192, p.wq); ld_nt<0>(wq2, owq2, p.wq);
  asm volatile("s_barrier");
  static_assert(WIN >= 3 && WIN <= 9 && WIN <= QPOS && QPOS >= 9, "first window");
  unsigned o0, ostep; const char* base0;
  if (A) {
    const int aidx = g * 9 + j;
    o0 = (unsigned)(144 * (aidx & 3) + 36 * (w & 3) + g4) * (F * 2) + ((aidx >> 2) * 128 + 8 * c) * 2;
    ostep = 4 * F * 2; base0 = w < 4 ? p.wg : p.wu;
  } else {
    o0 = kvmap(false, lane, w, g, j).o;
    ostep = KVSTEP; base0 = p.k;
  }
  v4u r0[WIN];
  sfor<0, WIN - 3>([&](auto N_) { ld_nt<0>(r0[N_], o0 + N_ * ostep, base0); });
  for (int i = 0; i < 4; ++i) pro.msk[i] = (i == (c8 >> 1)) ? ((lane & 1) ? 0xffff0000u : 0x0000ffffu) : 0u;

  land<WIN + 2>(xa);
  asm volatile("s_waitcnt vmcnt(%1)" : "+v"(xt) : "n"(WIN + 2));
  STAMP(1);
  *(v4u*)&L.x[lane * 8] = xa; L.x[512 + lane] = (unsigned short)xt;
  const unsigned u0 = L.x[wqrow0 + g4], u1 = L.x[wqrow0 + 4 + g4], u2 = L.x[wqrow0 + 8];
  {
    float xv[9];
    for (int i = 0; i < 4; ++i) { xv[2 * i] = asf(xa[i] << 16); xv[2 * i + 1] = asf(xa[i] & 0xffff0000u); }
    xv[8] = asf(xt << 16);
    const float mean = wavesum(((xv[0] + xv[1]) + (xv[2] + xv[3])) + ((xv[4] + xv[5]) + (xv[6] + xv[7])) + xv[8]) * (1.f / D);
    float s0 = 0.f, s1 = 0.f;
    for (int i = 0; i < 4; ++i) { const float a = xv[2 * i] - mean, b = xv[2 * i + 1] - mean; s0 += a * a; s1 += b * b; }
    const float t = xv[8] - mean;
    const float var = rbf(wavesum(s0 + s1 + t * t) * (1.f / D));
    pro.mb = rbf(mean);
    pro.rden = __builtin_amdgcn_rcpf(rbf(__builtin_amdgcn_sqrtf(rbf(var + 1e-5f))));
  }
  {
    const float y0 = rbf(asf(u0 << 16) - pro.mb) * pro.rden, y1 = rbf(asf(u1 << 16) - pro.mb) * pro.rden;
    const float y2 = g4 == 0 ? rbf(asf(u2 << 16) - pro.mb) * pro.rden : 0.f;
    const unsigned x0 = pk(y0, y0), x1 = pk(y1, y1), x2 = pk(y2, y2);
    land<WIN - 3>(wq2);
    asm volatile("" : "+v"(wq0), "+v"(wq1), "+v"(pro.epoch), "+v"(junk0));
    sfor<WIN - 3, WIN>([&](auto N_) { ld_nt<0>(r0[N_], o0 + N_ * ostep, base0); });
    STAMP(15);
    v4f a = {0.f, 0.f, 0.f, 0.f};
    a = mac(a, x0, pro.msk, wq0); a = mac(a, x1, pro.msk, wq1); a = mac(a, x2, pro.msk, wq2);
    if (lane < 32) *(v4f*)&L.rq[w][8 * c + 4 * g4] = a;
    wg_barrier();
    if (w == 0 && lane < 32) {
      v4f s = *(v4f*)&L.rq[0][4 * lane];
      for (int i = 1; i < 8; ++i) s += *(v4f*)&L.rq[i][4 * lane];
      st_dev(WS_QP + (unsigned)((g * 16 + j) * 512 + lane * 16), tagged(s, (pro.epoch + 1) & 0xffu), p.ws);
    }
    fence_sched();
  }
  STAMP(2);
  if (A) body<true>(p, L, pro, r0, o0, lane, w, g, j);
  else body<false>(p, L, pro, r0, o0, lane, w, g, j);
}

extern "C" void kernel_launch(void* const* d_in, const int* in_sizes, int n_in, void* d_out, int out_size,
                              void* d_ws, size_t ws_size, hipStream_t stream) {
  Params p;
  p.x = (const char*)d_in[0]; p.k = (const char*)d_in[1]; p.v = (const char*)d_in[2]; p.wq = (const char*)d_in[3];
  p.wo = (const char*)d_in[4]; p.wg = (const char*)d_in[6]; p.wu = (const char*)d_in[7]; p.wd = (const char*)d_in[8];
  p.ws = (char*)d_ws; p.out = (char*)d_out;
  fused_decode<<<256, 512, 0, stream>>>(p);
}
```

```cpp
#include <hip/hip_runtime.h>
#include <hip/hip_bf16.h>
#include <stdint.h>
#include <type_traits>

#define CA 120
#define DS 12
#define WIN 5
#define XA 1
#define XB 1
#define QPOS 10
#define HGP 2
#define PACE 480
#define PACEB 520
#define P0 6
#define ETA 2
#define ETB 2
#define EMA 1
#define EMB 1
#define XL 1
#define TLA 25, 50, 80, 95
#define TLB 25, 50, 80, 95, 110, 125
#define TG 130

#define GN 1
#define QXA 1
#define QXB 1
#define WKA WIN
#define WKB WKA
#define WVA WKA
#define WVB WKB

typedef unsigned v4u __attribute__((ext_vector_type(4)));
typedef float v4f __attribute__((ext_vector_type(4)));
typedef __bf16 bf16x2 __attribute__((ext_vector_type(2)));
typedef __bf16 bf16x8 __attribute__((ext_vector_type(8)));
#define DI __device__ __forceinline__
#define LDS __attribute__((address_space(3)))

constexpr int D = 576, SK = 8192, F = 4608;
constexpr int QUADS = SK / 4;
constexpr int NA = (CA + 7) / 8;
constexpr int NB = ((QUADS - 9 * CA + 3 * DS + 6) / 7 + 7) / 8;
static_assert(CA - DS >= 8 * (NA - 2) && (QUADS - 9 * CA + 2 * DS) / 7 >= 8 * (NB - 2), "key split");

constexpr unsigned WS_EPOCH = 0;
constexpr unsigned WS_QP = 4096;
constexpr unsigned WS_HP = WS_QP + 16 * 16 * 512;
constexpr unsigned WS_ML = WS_HP + 144 * 2048;
constexpr unsigned WS_ACC = WS_ML + 4096;
constexpr unsigned WS_SF = WS_ACC + 256 * 1024;
constexpr unsigned SF_STEP = 65536, SF_UNIT = 28 * 64;
constexpr int R2 = 4, TB = 10;
constexpr int SPIN_CAP = 1 << 16;
constexpr unsigned TAGM = (1u << TB) - 1;

struct Params {
  const char* x; const char* k; const char* v; const char* wq; const char* wo;
  const char* wg; const char* wu; const char* wd; char* ws; char* out;
};

template <int U, int N, class Fn> DI void sfor(Fn&& f) { if constexpr (U < N) { f(std::integral_constant<int, U>{}); sfor<U + 1, N>(f); } }
DI float asf(unsigned u) { return __builtin_bit_cast(float, u); }
DI unsigned asu(float f) { return __builtin_bit_cast(unsigned, f); }
DI unsigned pk(float lo, float hi) { bf16x2 r = {(__bf16)lo, (__bf16)hi}; return __builtin_bit_cast(unsigned, r); }
DI float rbf(float f) { return asf(pk(0.f, f)); }
DI void pinu(unsigned& r) { asm volatile("" : "+v"(r)); }
DI float dot2(unsigned a, unsigned b, float c) { return __builtin_amdgcn_fdot2_f32_bf16(__builtin_bit_cast(bf16x2, a), __builtin_bit_cast(bf16x2, b), c, false); }
template <int C> DI float dpp(float x) { return asf(__builtin_amdgcn_mov_dpp((int)asu(x), C, 0xf, 0xf, true)); }
constexpr int XOR1 = 0xB1, XOR2 = 0x4E, XOR7 = 0x141, XOR8 = 0x128;
DI float x16sum(float x) { auto s = __builtin_amdgcn_permlane16_swap(asu(x), asu(x), false, false); return asf(s[0]) + asf(s[1]); }
DI float x32sum(float x) { auto s = __builtin_amdgcn_permlane32_swap(asu(x), asu(x), false, false); return asf(s[0]) + asf(s[1]); }
DI float x16max(float x) { auto s = __builtin_amdgcn_permlane16_swap(asu(x), asu(x), false, false); return fmaxf(asf(s[0]), asf(s[1])); }
DI float x32max(float x) { auto s = __builtin_amdgcn_permlane32_swap(asu(x), asu(x), false, false); return fmaxf(asf(s[0]), asf(s[1])); }
DI float sum8(float x) { x += dpp<XOR1>(x); x += dpp<XOR2>(x); return x + dpp<XOR7>(x); }
DI float max8(float x) { x = fmaxf(x, dpp<XOR1>(x)); x = fmaxf(x, dpp<XOR2>(x)); return fmaxf(x, dpp<XOR7>(x)); }
DI float wavesum(float x) { x = sum8(x); x += dpp<XOR8>(x); return x32sum(x16sum(x)); }
DI float silu(float x) { return x * __builtin_amdgcn_rcpf(1.f + __expf(-x)); }
DI void fence_sched() { __builtin_amdgcn_sched_barrier(0); }
DI void wg_barrier() { asm volatile("s_waitcnt lgkmcnt(0)\n\ts_barrier" ::: "memory"); }

template <int OFF> DI void ld_nt(v4u& r, unsigned voff, const char* base) {
  asm volatile("global_load_dwordx4 %0, %1, %2 offset:%3 nt" : "=v"(r) : "v"(voff), "s"(base), "n"(OFF));
}
template <int OFF> DI void ld_dev(v4u& r, unsigned voff, const char* base) {
  asm volatile("global_load_dwordx4 %0, %1, %2 offset:%3 sc1" : "=v"(r) : "v"(voff), "s"(base), "n"(OFF));
}
DI void st_dev(unsigned voff, v4u d, char* base) {
  asm volatile("global_store_dwordx4 %0, %1, %2 sc1\n\ts_nop 1" :: "v"(voff), "v"(d), "s"(base) : "memory");
}
template <int N> DI void land(v4u& r) { asm volatile("s_waitcnt vmcnt(%1)" : "+v"(r) : "n"(N)); }
template <int N, int LIM> DI void land_at(v4u& r, unsigned t1) {
  asm volatile("s_memrealtime s[96:97]\n\ts_waitcnt vmcnt(%2) lgkmcnt(0)\n.Lat_chk%=:\n\ts_sub_u32 s96, s96, %1\n\ts_cmp_ge_u32 s96, %3\n\t"
               "s_cbranch_scc1 .Lat_go%=\n\ts_memrealtime s[96:97]\n\ts_waitcnt lgkmcnt(0)\n\ts_branch .Lat_chk%=\n.Lat_go%=:"
               : "+v"(r) : "s"(t1), "n"(N), "n"(LIM) : "scc", "s96", "s97");
}
template <int N, int LIM> DI void wait_at(unsigned t1) {
  asm volatile("s_memrealtime s[96:97]\n\ts_waitcnt vmcnt(%1) lgkmcnt(0)\n.Lat_chk%=:\n\ts_sub_u32 s96, s96, %0\n\ts_cmp_ge_u32 s96, %2\n\t"
               "s_cbranch_scc1 .Lat_go%=\n\ts_memrealtime s[96:97]\n\ts_waitcnt lgkmcnt(0)\n\ts_branch .Lat_chk%=\n.Lat_go%=:"
               :: "s"(t1), "n"(N), "n"(LIM) : "scc", "s96", "s97");
}
template <int N, int LIM> DI void gate_at(unsigned t1) {
  asm volatile("s_memrealtime s[96:97]\n\ts_waitcnt lgkmcnt(0)\n\ts_sub_u32 s96, s96, %0\n\ts_cmp_lt_u32 s96, %2\n\ts_cbranch_scc1 .Lg_spin%=\n\t"
               "s_waitcnt vmcnt(%1)\n\ts_branch .Lg_go%=\n.Lg_spin%=:\n\ts_memrealtime s[96:97]\n\ts_waitcnt lgkmcnt(0)\n\t"
               "s_sub_u32 s96, s96, %0\n\ts_cmp_lt_u32 s96, %2\n\ts_cbranch_scc1 .Lg_spin%=\n.Lg_go%=:"
               :: "s"(t1), "n"(N), "n"(LIM) : "scc", "s96", "s97");
}
template <int N> DI void wait_vm() { asm volatile("s_waitcnt vmcnt(%0)" :: "n"(N)); }
DI void pinf(v4f& r) { asm volatile("" : "+v"(r)); }
DI void pin(v4u& r) { asm volatile("" : "+v"(r)); }
DI v4u tagged(v4f v, unsigned tag) {
  v4u r; for (int i = 0; i < 4; ++i) r[i] = (asu(v[i]) & ~TAGM) | tag; return r;
}
DI v4f untag(v4u v) { v4f r; for (int i = 0; i < 4; ++i) r[i] = asf(v[i] & ~TAGM); return r; }
DI unsigned tagdiff(v4u v, unsigned tag) { return ((v[0] ^ tag) | (v[1] ^ tag) | (v[2] ^ tag) | (v[3] ^ tag)) & TAGM; }
DI bool all_ok(unsigned diff) { return __builtin_amdgcn_ballot_w64(diff != 0) == 0; }
DI void repoll1(v4u& a, unsigned oa, const char* ws, unsigned tag) {
  #pragma unroll 1
  for (int s = 0; s < SPIN_CAP && !all_ok(tagdiff(a, tag)); ++s) { ld_dev<0>(a, oa, ws); land<0>(a); }
}
DI void repoll2(v4u& a, v4u& b, unsigned oa, unsigned ob, const char* ws, unsigned tag) {
  #pragma unroll 1
  for (int s = 0; s < SPIN_CAP && !all_ok(tagdiff(a, tag) | tagdiff(b, tag)); ++s) {
    ld_dev<0>(a, oa, ws); ld_dev<0>(b, ob, ws); land<0>(a); land<0>(b);
  }
}
#define POLL_SEND \
  "global_load_dwordx4 v[156:159], %[oa], %[ws] sc1\n\tglobal_load_dwordx4 v[160:163], %[ob], %[ws] sc1\n\t" \
  "s_sub_u32 %[k], %[k], 1\n\ts_cselect_b32 %[k], 0, %[k]\n\ts_cselect_b32 %[st], 0, %[st]\n\t" \
  "v_add_u32 %[oa], %[st], %[oa]\n\tv_add_u32 %[ob], %[st], %[ob]\n\t"
#define POLL_TEST \
  "s_waitcnt vmcnt(0)\n\t" \
  "v_bitop3_b32 %[t0], v156, v157, %[tag] bitop3:0x7e\n\tv_bitop3_b32 %[t1], v158, v159, %[tag] bitop3:0x7e\n\t" \
  "v_bitop3_b32 %[t2], v160, v161, %[tag] bitop3:0x7e\n\tv_bitop3_b32 %[t3], v162, v163, %[tag] bitop3:0x7e\n\t" \
  "v_or3_b32 %[t4], %[t0], %[t1], %[t2]\n\tv_bitop3_b32 %[t4], %[t4], %[t3], %[ff] bitop3:0xa8\n\tv_cmp_ne_u32 vcc, 0, %[t4]\n\t"
DI void poll2(v4u& a, v4u& b, unsigned oa, unsigned ob, unsigned step, const char* ws, unsigned tag) {
  unsigned k = R2 - 1, n = SPIN_CAP, t0, t1, t2, t3, t4;
  asm volatile(POLL_SEND ".Lpoll_loop%=:\n\t" POLL_TEST "s_cbranch_vccz .Lpoll_done%=\n\t" POLL_SEND
    "s_sub_u32 %[n], %[n], 1\n\ts_cbranch_scc0 .Lpoll_loop%=\n\ts_waitcnt vmcnt(0)\n.Lpoll_done%=:"
    : "=&{v[156:159]}"(a), "=&{v[160:163]}"(b), [oa] "+v"(oa), [ob] "+v"(ob), [k] "+s"(k), [st] "+s"(step), [n] "+s"(n),
      [t0] "=&v"(t0), [t1] "=&v"(t1), [t2] "=&v"(t2), [t3] "=&v"(t3), [t4] "=&v"(t4)
    : [ws] "s"(ws), [tag] "v"(tag), [ff] "s"(TAGM) : "vcc", "scc");
}
DI unsigned mpoll2(v4u& a, v4u& b, unsigned oa, unsigned ob, const char* ws, unsigned tag) {
  unsigned n = SPIN_CAP, t0, t1, t2, t3, t4; unsigned long long m;
  asm volatile(
    "global_load_dwordx4 v[156:159], %[oa], %[ws] sc1\n\tglobal_load_dwordx4 v[160:163], %[ob], %[ws] sc1\n"
    ".Lmp_loop%=:\n\t" POLL_TEST "s_cbranch_vccz .Lmp_done%=\n\t"
    "v_bitop3_b32 %[t0], %[t0], %[t1], %[ff] bitop3:0xa8\n\tv_bitop3_b32 %[t2], %[t2], %[t3], %[ff] bitop3:0xa8\n\t"
    "v_cmp_ne_u32 vcc, 0, %[t0]\n\tv_cmp_ne_u32 %[m], 0, %[t2]\n\t"
    "s_mov_b64 exec, vcc\n\ts_cbranch_execz .Lmp_b%=\n\tglobal_load_dwordx4 v[156:159], %[oa], %[ws] sc1\n"
    ".Lmp_b%=:\n\ts_mov_b64 exec, %[m]\n\ts_cbranch_execz .Lmp_n%=\n\tglobal_load_dwordx4 v[160:163], %[ob], %[ws] sc1\n"
    ".Lmp_n%=:\n\ts_mov_b64 exec, -1\n\ts_sub_u32 %[n], %[n], 1\n\ts_cbranch_scc0 .Lmp_loop%=\n\t"
    "s_waitcnt vmcnt(0)\n.Lmp_done%=:"
    : "=&{v[156:159]}"(a), "=&{v[160:163]}"(b), [n] "+s"(n), [t0] "=&v"(t0), [t1] "=&v"(t1), [t2] "=&v"(t2), [t3] "=&v"(t3),
      [t4] "=&v"(t4), [m] "=&s"(m)
    : [oa] "v"(oa), [ob] "v"(ob), [ws] "s"(ws), [tag] "v"(tag), [ff] "s"(TAGM) : "vcc", "scc");
  return SPIN_CAP - n;
}
#define QUAD_SCATTER \
  "v_add_f32_dpp v140, v132, v132 row_ror:8 row_mask:0xf bank_mask:0x3\n\tv_add_f32_dpp v140, v134, v134 row_ror:8 row_mask:0xf bank_mask:0xc\n\t" \
  "v_add_f32_dpp v141, v133, v133 row_ror:8 row_mask:0xf bank_mask:0x3\n\tv_add_f32_dpp v141, v135, v135 row_ror:8 row_mask:0xf bank_mask:0xc\n\t" \
  "v_add_f32_dpp v142, v140, v140 row_shl:4 row_mask:0xf bank_mask:0x5\n\tv_add_f32_dpp v143, v140, v140 row_shl:4 row_mask:0xf bank_mask:0x5\n\t" \
  "v_add_f32_dpp v142, v141, v141 row_shr:4 row_mask:0xf bank_mask:0xa\n\tv_add_f32_dpp v143, v141, v141 row_shr:4 row_mask:0xf bank_mask:0xa\n\ts_nop 1\n\t"
#define SCATTER_REGS "v132", "v133", "v134", "v135", "v136", "v137", "v138", "v139", "v140", "v141", "v142", "v143", "memory"
template <int GAP> DI void publish32(const v4f a, unsigned lw, unsigned lr, unsigned off, char* ws, unsigned tag, unsigned nm) {
  asm volatile("s_nop %[k]\n\tds_write_b128 %[lw], %[a]\n\ts_waitcnt lgkmcnt(0)\n\ts_barrier\n\t"
    "ds_read_b128 v[132:135], %[lr]\n\ts_waitcnt lgkmcnt(0)\n\t" QUAD_SCATTER
    "v_permlane32_swap_b32 v142, v143\n\ts_mov_b32 exec_hi, 0\n\tv_add_f32 v142, v142, v143\n\tv_and_or_b32 v142, v142, %[nm], %[tag]\n\t"
    "global_store_dword %[off], v142, %[ws] sc1\n\ts_mov_b32 exec_hi, -1"
    :: [a] "v"(a), [lw] "v"(lw), [lr] "v"(lr), [off] "v"(off), [ws] "s"(ws), [nm] "s"(nm), [tag] "v"(tag), [k] "n"(7 - GAP) : SCATTER_REGS);
}
struct Pub { unsigned lw, lr, off, tag, nm; int on; char* ws; };
#define PUB_TEXT \
  "s_mov_b32 exec_lo, %[lo]\n\ts_mov_b32 exec_hi, %[hi]\n\ts_cmp_eq_u32 %[on], 0\n\ts_nop 4\n\tds_write_b128 %[lw], %[s]\n\t" \
  "s_mov_b64 exec, -1\n\ts_cbranch_scc1 .Lpub_idle%=\n\ts_waitcnt lgkmcnt(0)\n\ts_barrier\n\t" \
  "ds_read_b128 v[132:135], %[lr]\n\tds_read_b128 v[136:139], %[lr] offset:%[o2]\n\ts_waitcnt lgkmcnt(0)\n\t" \
  "v_add_f32 v132, v132, v136\n\tv_add_f32 v134, v134, v138\n\tv_add_f32 v133, v133, v137\n\tv_add_f32 v135, v135, v139\n\t" QUAD_SCATTER \
  "v_permlane16_swap_b32 v142, v143\n\tv_add_f32 v142, v142, v143\n\tv_and_or_b32 v142, v142, %[nm], %[tag]\n\t" \
  "global_store_dword %[off], v142, %[ws] sc1\n\ts_branch .Lpub_done%=\n" \
  ".Lpub_idle%=:\n\ts_waitcnt lgkmcnt(0)\n\ts_barrier\n.Lpub_done%=:"
#define PUB_ARGS(LO, HI) \
  [lw] "v"(P.lw), [lr] "v"(P.lr), [off] "v"(P.off), [ws] "s"(P.ws), [nm] "s"(P.nm), [tag] "v"(P.tag), [on] "s"(P.on), \
  [lo] "n"(LO), [hi] "n"(HI), [o2] "n"(8 * RS_ROW * 4)
constexpr int RS_ROW = 80;
DI void finish(v4u a, v4u b, unsigned nm, unsigned fm, float xr, unsigned off, char* out, unsigned long long lanes) {
  float y, e, s;
  asm volatile(
    "v_and_b32 v156, %[nm], v156\n\tv_and_b32 v160, %[fm], v160\n\tv_and_b32 v158, %[nm], v158\n\tv_and_b32 v162, %[fm], v162\n\t"
    "v_and_b32 v157, %[nm], v157\n\tv_and_b32 v161, %[fm], v161\n\tv_and_b32 v159, %[nm], v159\n\tv_and_b32 v163, %[fm], v163\n\t"
    "v_permlane32_swap_b32 v156, v160\n\tv_permlane32_swap_b32 v158, v162\n\tv_permlane32_swap_b32 v157, v161\n\tv_permlane32_swap_b32 v159, v163\n\t"
    "v_add_f32 v156, v156, v160\n\tv_add_f32 v158, v158, v162\n\tv_add_f32 v157, v157, v161\n\tv_add_f32 v159, v159, v163\n\t"
    "v_permlane16_swap_b32 v156, v158\n\tv_add_f32 v156, v156, v158\n\tv_permlane16_swap_b32 v157, v159\n\tv_add_f32 v157, v157, v159\n\t"
    "v_add_f32_dpp %[y], v156, v156 row_ror:8 row_mask:0xf bank_mask:0x3\n\ts_nop 0\n\t"
    "v_add_f32_dpp %[y], v157, v157 row_ror:8 row_mask:0xf bank_mask:0xc\n\ts_nop 1\n\t"
    "v_add_f32_dpp %[y], %[y], %[y] row_half_mirror row_mask:0xf bank_mask:0xf\n\tv_cvt_pk_bf16_f32 %[y], 0, %[y]\n\t"
    "v_mul_f32 %[e], 0xbfb8aa3b, %[y]\n\tv_exp_f32 %[e], %[e]\n\tv_permlane32_swap_b32 %[s], %[y]\n\t"
    "v_add_f32 %[e], 1.0, %[e]\n\tv_rcp_f32 %[e], %[e]\n\ts_mov_b64 exec, %[lanes]\n\t"
    "v_mul_f32 %[e], %[e], %[y]\n\tv_cvt_pk_bf16_f32 %[e], 0, %[e]\n\tv_mul_f32 %[e], %[s], %[e]\n\tv_cvt_pk_bf16_f32 %[e], 0, %[e]\n\t"
    "v_add_f32 %[e], %[xr], %[e]\n\tv_cvt_pk_bf16_f32 %[e], %[e], %[e]\n\tglobal_store_short %[off], %[e], %[out]\n\ts_mov_b64 exec, -1"
    : "+{v[156:159]}"(a), "+{v[160:163]}"(b), [y] "=&v"(y), [e] "=&v"(e), [s] "=&v"(s)
    : [nm] "s"(nm), [fm] "v"(fm), [xr] "v"(xr), [off] "v"(off), [out] "s"(out), [lanes] "s"(lanes) : "memory");
}
DI float rowscatter(const v4f v) {
  float t[2];
  for (int i = 0; i < 2; ++i) { auto x = __builtin_amdgcn_permlane32_swap(asu(v[i]), asu(v[i + 2]), false, false); t[i] = asf(x[0]) + asf(x[1]); }
  auto x = __builtin_amdgcn_permlane16_swap(asu(t[0]), asu(t[1]), false, false);
  return asf(x[0]) + asf(x[1]);
}
template <int LO, int HI> DI unsigned halfcast(unsigned x) {
  const int t = __builtin_amdgcn_update_dpp(0, (int)x, 0x150 + LO, 0xf, 0x3, false);
  return (unsigned)__builtin_amdgcn_update_dpp(t, (int)x, 0x150 + HI, 0xf, 0xc, false);
}
DI v4u spread(unsigned dup, const v4u& msk) { return v4u{dup & msk[0], dup & msk[1], dup & msk[2], dup & msk[3]}; }
DI v4f mma(v4f acc, const v4u& a, const v4u& w) {
  return __builtin_amdgcn_mfma_f32_16x16x32_bf16(__builtin_bit_cast(bf16x8, a), __builtin_bit_cast(bf16x8, w), acc, 0, 0, 0);
}
template <int N> DI void mma_landed(v4f& acc, const v4u& a, const v4u& w) {
  asm volatile("s_waitcnt vmcnt(%3)\n\tv_mfma_f32_16x16x32_bf16 %0, %1, %2, %0" : "+v"(acc) : "v"(a), "v"(w), "n"(N));
}
DI v4f mac(v4f acc, unsigned dup, const v4u& msk, const v4u& w) { return mma(acc, spread(dup, msk), w); }

#define PRJ_AND(R, I, M) "v_and_b32_dpp v" #R ", %[o], %[m" #M "] quad_perm:[" #I "," #I "," #I "," #I "] row_mask:0xf bank_mask:0xf\n\t"
DI void project4(float o, const v4u& msk, const v4u& w0, const v4u& w1, const v4u& w2, const v4u& w3, const Pub& P) {
  v4f s;
  asm volatile("s_nop 1\n\tv_add_f32_dpp %[o], %[o], %[o] row_ror:8 row_mask:0xf bank_mask:0xf\n\tv_cvt_pk_bf16_f32 %[o], %[o], %[o]\n\ts_nop 1\n\t"
    PRJ_AND(132, 0, 0) PRJ_AND(133, 0, 1) PRJ_AND(134, 0, 2) PRJ_AND(135, 0, 3) PRJ_AND(136, 1, 0) PRJ_AND(137, 1, 1)
    "v_mfma_f32_16x16x32_bf16 %[s], v[132:135], %[w0], 0\n\t"
    PRJ_AND(138, 1, 2) PRJ_AND(139, 1, 3) PRJ_AND(140, 2, 0) PRJ_AND(141, 2, 1)
    "v_mfma_f32_16x16x32_bf16 %[s], v[136:139], %[w1], %[s]\n\t"
    PRJ_AND(142, 2, 2) PRJ_AND(143, 2, 3) PRJ_AND(144, 3, 0) PRJ_AND(145, 3, 1) PRJ_AND(146, 3, 2) PRJ_AND(147, 3, 3)
    "v_mfma_f32_16x16x32_bf16 %[s], v[140:143], %[w2], %[s]\n\ts_nop 0\n\t"
    "v_mfma_f32_16x16x32_bf16 %[s], v[144:147], %[w3], %[s]\n\t" PUB_TEXT
    : [s] "=&v"(s), [o] "+v"(o)
    : [m0] "v"(msk[0]), [m1] "v"(msk[1]), [m2] "v"(msk[2]), [m3] "v"(msk[3]), [w0] "v"(w0), [w1] "v"(w1), [w2] "v"(w2), [w3] "v"(w3),
      PUB_ARGS(0xff0000ffu, 0xff0000ffu)
    : SCATTER_REGS, "v144", "v145", "v146", "v147", "scc");
}
DI void project_last(v4f s, const v4u& a, const v4u& w, const Pub& P) {
  asm volatile("s_waitcnt vmcnt(0)\n\tv_mfma_f32_16x16x32_bf16 %[s], %[a], %[w], %[s]\n\t" PUB_TEXT
    : [s] "+v"(s) : [a] "v"(a), [w] "v"(w), PUB_ARGS(0x00ff00ffu, 0xff00ff00u) : SCATTER_REGS, "scc");
}

struct Lds {
  unsigned short x[D];
  float rq[8][128];
  float rg[8][128];
  float rqg[8][256];
  unsigned qb[128];
  float mx[2][8];
  float ls[2][8];
  float ra[8][272];
  float rs[16][RS_ROW];
};

#define STAMP(i) do {} while (0)

template <bool A> struct Ops {
  static constexpr int NK = A ? NA : NB, NG = (NK + 7) / 8;
  static constexpr int iK = A ? 9 : 0, iQG = QPOS, iV = iK + NK + 1;
  static constexpr int XP = A ? NK : HGP, NX = A ? 1 : 2, NT = A ? 4 : 6;
  static constexpr int iX = iV + XP, iT = iV + NK + NX, N = iT + NT;
  static constexpr int kidx(int i) { return iK + i + (iK + i >= iQG ? 1 : 0); }
  static constexpr int vidx(int i) { return iV + i + (i >= XP ? NX : 0); }
  static constexpr bool gather(int n) { return n == iQG || (n >= iX && n < iX + NX); }
  static constexpr int X = A ? XA : XB, ET = A ? ETA : ETB, EM = A ? EMA : EMB, QX = A ? QXA : QXB, PC = A ? PACE : PACEB, nL = vidx(NK - 1);
  static constexpr int ahead(int k, int n) { int h = k; for (int c = 0; c < n; ++h) if (!gather(h)) ++c; return h; }
  static constexpr int slots(int n) { int c = 0; for (int h = 0; h < n; ++h) if (!gather(h)) ++c; return c; }
  static constexpr int due(int n) {
    constexpr int ta[6] = {TLA}, tb[6] = {TLB};
    if (n > nL) return due(nL) + (n < iT ? 0 : A ? ta[n - iT] : tb[n - iT]);
    return (slots(n) - 2) * PC >> 4;
  }
  static constexpr int WK = A ? WKA : WKB, WV = A ? WVA : WVB;
  static constexpr int win(int k) {
    if (k > iV) return WK + k - iV < WV ? WK + k - iV : WV;
    if (k > iQG + 1) return WIN + k - iQG - 1 < WK ? WIN + k - iQG - 1 : WK;
    return WIN;
  }
  static constexpr int head(int k) {
    int h = ahead(k, win(k));
    if (k > iQG && h < ahead(iQG, WIN + QX)) h = ahead(iQG, WIN + QX);
    if (k >= iV && h < ahead(iV, WK + X)) h = ahead(iV, WK + X);
    if (k >= nL) h = N;
    if (k <= nL && h > iT + ET) h = iT + ET;
    if (h > iT + ET + EM) h = iT + ET + EM;
    return h < N ? h : N;
  }
  static constexpr int first(int k) { int h = head(k); while (gather(h) && h + 1 < head(k + 1)) ++h; return h; }
};

constexpr unsigned KVSTEP = 8192;
struct KvMap { unsigned o, last, prev; int nvalid; };
DI KvMap kvmap(const bool A, const int lane, const int w, const int g, const int j) {
  const int jb = j - 9, nk = A ? NA : NB, s8 = (g & 3) == 1 ? DS : 0;
  const int qa = 9 * CA - 2 * DS - s8, blo = (QUADS - qa) / 7, bex = QUADS - qa - 7 * blo;
  const int cnt = A ? CA - (j == 8 ? s8 : (j & 3) == 1 ? DS : 0) : blo + (jb < bex ? 1 : 0);
  const int quad0 = (A ? j * CA - ((j + 2) >> 2) * DS : qa + jb * blo + (jb < bex ? jb : bex)) + w;
  const int nvalid = (cnt - w + 7) >> 3;
  const unsigned row0 = (unsigned)(g * SK + quad0 * 4) * 256u, o = row0 + lane * 16;
  return {o, nvalid >= nk ? o + (nk - 1) * KVSTEP : row0, nvalid >= nk - 1 ? o + (nk - 2) * KVSTEP : row0, nvalid};
}
DI int gutask(const int g, const int j) {
  const int cls = j < 8 ? j & 3 : g & 3, n = j < 8 ? 2 * g + (j >> 2) : 32 + (g >> 2);
  return ((n & ~3) + cls) * 4 + (n & 3);
}

struct Pro {
  v4u msk; unsigned epoch, t1; float mb, rden;
};

template <bool A> DI void body(const Params& p, Lds& L, Pro& pro, v4u (&r0)[WIN], const unsigned o0,
                               const int lane, const int w, const int g, const int j) {
  using O = Ops<A>;
  constexpr int NK = O::NK, NG = O::NG, iK = O::iK, iQG = O::iQG, iV = O::iV;
  constexpr int iX = O::iX, NX = O::NX, iT = O::iT, NOPS = O::N;
  const int c = lane & 15, g4 = lane >> 4, r8 = lane >> 3, c8 = lane & 7, aidx = gutask(g, j);
  const v4u msk = pro.msk;
  const unsigned tag = (pro.epoch + 1) & TAGM;
  v4u r[NOPS];
  sfor<0, WIN>([&](auto N_) { r[N_] = r0[N_]; });

  const KvMap km = kvmap(A, lane, w, g, j);
  const unsigned okv = km.o, okv_last = km.last, okv_prev = km.prev;
  const int nvalid = km.nvalid;
  const unsigned oqg = WS_QP + (unsigned)((g * 16 + 2 * w) * 512 + lane * 16);
  const int gurow0 = 144 * (aidx & 3) + 36 * (w & 3);
  const char* wgu = w >> 2 ? p.wu : p.wg;
  const int bidx = g * 7 + (j - 9), part = bidx / 9, cb = A ? j : bidx - 9 * part;
  const bool tail_on = A || bidx < 108;
  const int trow = A ? 256 * g + 32 * w : (tail_on ? 384 * part + 48 * w : 0);
  const int hb = (lane >> 3) & 1;
  const unsigned ot0 = tail_on ? (unsigned)(trow + (A ? 4 : 16) * hb + g4) * (D * 2) + (64 * cb + 8 * c8) * 2 : 0u;
  const unsigned ot1 = tail_on ? (unsigned)(trow + 32 + 8 * hb + g4) * (D * 2) + (64 * cb + 8 * c8) * 2 : 0u;
  auto hpoff = [&](int col) { return WS_HP + (unsigned)((((col >> 7) * 4 + g4) * 2 + hb) * 512 + (col & 127) * 4); };
  const unsigned ox0 = A ? WS_ML + (unsigned)((g * 16 + r8 + 2 * (lane & 4)) * 16) : hpoff(trow + 4 * c8);
  const unsigned ox1 = hpoff(trow + 32 + 4 * (lane & 3));

  auto issue = [&](auto N_) {
    constexpr int n = N_;
    if constexpr (n < iK) {
      ld_nt<0>(r[n], o0 + n * 4 * (F * 2), wgu);
    } else if constexpr (n == iQG) {
      ld_dev<0>(r[n], oqg, p.ws);
    } else if constexpr (n >= iX && n < iX + NX) {
      ld_dev<0>(r[n], n == iX ? ox0 : ox1, p.ws);
    } else if constexpr (n < iT) {
      constexpr bool isk = n < iV;
      constexpr int i = isk ? n - iK - (n > iQG ? 1 : 0) : n - iV - (n > iX ? NX : 0);
      const char* base = isk ? p.k : p.v;
      if constexpr (i == NK - 1) ld_nt<0>(r[n], okv_last, base);
      else if constexpr (i == NK - 2) ld_nt<0>(r[n], okv_prev, base);
      else ld_nt<0>(r[n], okv, base + i * KVSTEP);
    } else {
      constexpr int i = n - iT;
      if constexpr (i < 4) ld_nt<0>(r[n], ot0, (A ? p.wo : p.wd) + i * (A ? 8 : 4) * D * 2); else ld_nt<0>(r[n], ot1, p.wd + (i - 4) * 4 * D * 2);
    }
  };
  auto step = [&](auto K_) {
    constexpr int k = K_;
    constexpr bool paced = k >= P0 && k != O::nL && k != iQG && O::head(k + 1) > O::head(k);
    if constexpr (k < WIN) wait_vm<O::head(k) - 1 - k>();
    else if constexpr (paced) land_at<O::head(k) - 1 - k, O::due(O::first(k))>(r[k], pro.t1);
    else land<O::head(k) - 1 - k>(r[k]);
    if constexpr (k + 1 == iV) issue(std::integral_constant<int, O::head(k)>{});
    else if constexpr (k != iQG) sfor<O::head(k), O::head(k + 1)>([&](auto N_) {
      if constexpr (paced && N_ > O::first(k) && !O::gather(N_)) wait_at<63, O::due(N_)>(pro.t1);
      issue(N_);
    });
  };

  if constexpr (A) {
    unsigned xr[9];
    for (int i = 0; i < 9; ++i) { const float y = rbf(asf((unsigned)L.x[gurow0 + 4 * i + g4] << 16) - pro.mb) * pro.rden; xr[i] = pk(y, y); }
    v4f a = {0.f, 0.f, 0.f, 0.f};
    sfor<0, 9>([&](auto I_) {
      constexpr int i = I_;
      step(I_);
      pinf(a);
      a = mac(a, xr[i], msk, r[i]);
      fence_sched();
    });
    if (lane < 32) *(v4f*)&L.rg[w][8 * c + 4 * g4] = a;
    wg_barrier();
    if (w == 2) {
      const int mt = lane >> 5, q = lane & 31;
      v4f s = *(v4f*)&L.rg[4 * mt][4 * q];
      for (int i = 1; i < 4; ++i) s += *(v4f*)&L.rg[4 * mt + i][4 * q];
      st_dev(WS_HP + (unsigned)(aidx * 1024 + lane * 16), tagged(s, tag), p.ws);
    }
    fence_sched();
  }
  STAMP(6);
  sfor<iK, iQG + 1>(step);
  STAMP(3);
  v4u qa, qb;
  {
    repoll1(r[iQG], oqg, p.ws, tag);
    *(v4f*)&L.rqg[w][4 * lane] = untag(r[iQG]);
    constexpr int q0 = O::head(iQG), q1 = O::head(iQG + 1);
    wg_barrier();
    sfor<q0, q1>(issue);
    v4f s = *(v4f*)&L.rqg[0][4 * lane];
    for (int i = 1; i < 8; ++i) s += *(v4f*)&L.rqg[i][4 * lane];
    *(uint2*)&L.qb[2 * lane] = make_uint2(pk(s[0], s[1]), pk(s[2], s[3]));
    qa = *(v4u*)&L.qb[hb * 64 + 4 * c]; qb = *(v4u*)&L.qb[(1 - hb) * 64 + 4 * c];
    fence_sched();
  }
  STAMP(4);
  float sc[NG], t[8];
  auto score = [&](auto Q_) {
    constexpr int q = Q_, G = q >> 3, i = q & 7, n = (NK - 8 * G) < 8 ? (NK - 8 * G) : 8;
    const v4u kk = r[O::kidx(q)];
    float a = 0.f, b = 0.f;
    for (int e = 0; e < 4; ++e) { a = dot2(kk[e], qa[e], a); b = dot2(kk[e], qb[e], b); }
    t[i] = a + dpp<XOR8>(b);
    if constexpr (i == n - 1) {
      auto fold = [&](auto H_, auto C_, auto N_) {
        constexpr int H = H_, C = C_, m = N_;
        sfor<0, H>([&](auto I_) {
          constexpr int i = I_;
          if constexpr (i + H < m) { const float lo = t[i] + dpp<C>(t[i]), hi = t[i + H] + dpp<C>(t[i + H]); t[i] = (lane & H) ? hi : lo; }
          else if constexpr (i < m) t[i] = t[i] + dpp<C>(t[i]);
        });
      };
      using std::integral_constant;
      fold(integral_constant<int, 4>{}, integral_constant<int, XOR7>{}, integral_constant<int, n>{});
      fold(integral_constant<int, 2>{}, integral_constant<int, XOR2>{}, integral_constant<int, (n < 4 ? n : 4)>{});
      fold(integral_constant<int, 1>{}, integral_constant<int, XOR1>{}, integral_constant<int, (n < 2 ? n : 2)>{});
      const float s = rbf(rbf(t[0]) * 0.08838834764831845f);
      sc[G] = (8 * G + c8 < nvalid) ? s : -INFINITY;
    }
  };
  constexpr int i0 = iQG - iK;
  static_assert(3 * (NK - i0) >= NK, "score backlog");
  sfor<i0, NK>([&](auto I_) {
    constexpr int i = I_, lo = i == i0 ? 0 : (i < 3 * (i - i0) ? i : 3 * (i - i0)), hi = i + 1 < 3 * (i - i0 + 1) ? i + 1 : 3 * (i - i0 + 1);
    step(std::integral_constant<int, O::kidx(i)>{});
    sfor<lo, hi>(score);
    fence_sched();
  });
  STAMP(14);
  constexpr int x0 = O::head(iV - 1) + 1, x1 = O::head(iV), xm = (x0 + x1 + 1) / 2;
  unsigned pd[NG];
  {
    float m = sc[0];
    for (int G = 1; G < NG; ++G) m = fmaxf(m, sc[G]);
    m = x32max(x16max(max8(m)));
    if ((lane & 0x37) == 0) L.mx[hb][w] = m;
    wg_barrier();
    sfor<x0, xm>(issue);
    const v4f m0 = *(v4f*)&L.mx[hb][0], m1 = *(v4f*)&L.mx[hb][4];
    m = fmaxf(fmaxf(fmaxf(m0[0], m0[1]), fmaxf(m0[2], m0[3])), fmaxf(fmaxf(m1[0], m1[1]), fmaxf(m1[2], m1[3])));
    float l = 0.f;
    for (int G = 0; G < NG; ++G) { const float e = __expf(sc[G] - m); l += e; pd[G] = pk(e, e); }
    l = x32sum(x16sum(sum8(l)));
    if ((lane & 0x37) == 0) L.ls[hb][w] = l;
    wg_barrier();
    sfor<xm, x1>(issue);
    if (w == 1) {
      const v4f l0 = *(v4f*)&L.ls[hb][0], l1 = *(v4f*)&L.ls[hb][4];
      l = ((l0[0] + l0[1]) + (l0[2] + l0[3])) + ((l1[0] + l1[1]) + (l1[2] + l1[3]));
      const v4f ml = {m, l, dpp<XOR8>(m), dpp<XOR8>(l)};
      if (lane == 0) st_dev(WS_ML + (unsigned)((g * 16 + j) * 16), tagged(ml, tag), p.ws);
    }
    fence_sched();
  }
  STAMP(5);
  float w0 = 0.f, w1 = 0.f;
  unsigned hd[6];
  auto extras = [&]() {
    if constexpr (A) {
      pin(r[iX]);
      repoll1(r[iX], ox0, p.ws, tag);
      const v4f a = untag(r[iX]);
      const float m = w < 4 ? a[0] : a[2], l = w < 4 ? a[1] : a[3];
      float M = fmaxf(m, dpp<XOR7>(m)); M = x32max(x16max(fmaxf(M, dpp<XOR8>(M))));
      const float e = __expf(m - M);
      float t = e * l; t += dpp<XOR7>(t); t = x32sum(x16sum(t + dpp<XOR8>(t)));
      const float own = e * __builtin_amdgcn_rcpf(t), other = dpp<XOR7>(own);
      w0 = (lane & 4) ? other : own; w1 = (lane & 4) ? own : other;
    } else {
      sfor<iX, iX + NX>(step);
      pin(r[iX]);
      repoll2(r[iX], r[iX + 1], ox0, ox1, p.ws, tag);
      const float v = rowscatter(untag(r[iX])), z = rowscatter(untag(r[iX + 1]));
      const float o = dpp<XOR8>(hb ? v : z);
      const float h = rbf(silu(rbf(hb ? o : v))) * rbf(hb ? z : o);
      const unsigned hh = pk(h, h);
      sfor<0, 4>([&](auto I_) { hd[I_] = halfcast<I_, I_ + 4>(hh); });
      sfor<0, 2>([&](auto I_) { hd[4 + I_] = halfcast<8 + I_, 10 + I_>(hh); });
    }
    fence_sched();
    STAMP(13);
  };
  const int b4 = (lane >> 4) & 1, pp = (lane >> 2) & 3, q = lane & 3;
  const int sh = A ? g4 & 1 : g4 >> 1, sq = A ? g4 >> 1 : g4 & 1;
  unsigned lw = (unsigned)(size_t)(LDS float*)&L.rs[2 * w + sh][8 * c8 + 4 * sq];
  unsigned lr = (unsigned)(size_t)(LDS float*)&L.rs[pp + 4 * b4][16 * (w & 3) + 4 * q];
  unsigned os = WS_SF + (unsigned)(g4 * SF_STEP + (4 * cb + (w & 3)) * SF_UNIT + (A ? g : 16 + part) * 64 + q * 16 + pp * 4);
  unsigned nm = ~TAGM;
  int pub = A ? (w >> 2) ^ 1 : (w >> 2) & (int)((unsigned)(bidx - 108) >> 31);
  {
    unsigned aw = (unsigned)(size_t)(LDS float*)&L.ra[w][(g4 >> 1) * 128 + (g4 & 1) * 64 + 4 * c];
    unsigned ar = (unsigned)(size_t)(LDS float*)&L.ra[pp + 4 * (lane >> 5)][(w >> 2) * 128 + b4 * 64 + (w & 3) * 16 + 4 * q];
    unsigned oa = WS_ACC + (unsigned)((g * 16 + j) * 1024 + w * 128 + b4 * 64 + q * 16 + pp * 4);
    constexpr int TSTEP = (A ? 8 : 4) * D * 2;
    const char* wem = (A ? p.wo : p.wd) + O::ET * TSTEP;
    v4f a = {0.f, 0.f, 0.f, 0.f};
    sfor<0, NK>([&](auto I_) {
      constexpr int i = I_;
      if constexpr (i == O::XP) extras();
      v4u pa = spread(halfcast<(i & 7), 8 + (i & 7)>(pd[i >> 3]), msk);
      pin(pa);
      if constexpr (i == NK - 6) {
        pinu(aw); pinu(ar); pinu(oa); pinu(lw); pinu(lr); pinu(os);
        asm volatile("" : "+s"(nm), "+s"(pub), "+s"(wem));
      }
      if constexpr (i == NK - 1) {
        asm volatile("s_nop 0");
        mma_landed<O::head(O::nL) - 1 - O::nL>(a, pa, r[O::nL]);
      } else {
        step(std::integral_constant<int, O::vidx(i)>{});
        a = mma(a, pa, r[O::vidx(i)]);
      }
      fence_sched();
    });
    static_assert(O::head(O::nL) == iT + O::ET && O::ET + O::EM <= 4, "loads at the last landing");
    sfor<0, O::EM>([&](auto I_) { ld_nt<0>(r[iT + O::ET + I_], ot0, wem + I_ * TSTEP); });
    STAMP(7);
    publish32<O::head(O::nL + 1) - O::head(O::nL)>(a, aw, ar, oa, p.ws, tag, nm);
    STAMP(8);
    fence_sched();
  }
  auto late = [&](auto N_) { wait_at<63, O::due(N_)>(pro.t1); issue(N_); };
  constexpr int l0 = iT + O::ET + O::EM, l1 = A && l0 + XL < NOPS ? l0 + XL : NOPS;
  if constexpr (A) {
    wait_vm<O::ET + O::EM + 1>();
    sfor<l0, l1>(late);
    extras();
    sfor<l1, NOPS>(late);
    v4u msk2;
    for (int i = 0; i < 4; ++i) msk2[i] = (i == (((c >> 1) & 1) | ((c >> 3) << 1))) ? ((lane & 1) ? 0xffff0000u : 0x0000ffffu) : 0u;
    unsigned oac = WS_ACC + (unsigned)((g * 16 + r8) * 1024 + (32 * w + 4 * c8) * 4), oad = oac + 8 * 1024;
    pin(msk2); pinu(oac); pinu(oad);
    gate_at<GN, O::due(O::nL) + TG>(pro.t1);
    STAMP(9);
    v4u pa, pb;
    const unsigned rounds = mpoll2(pa, pb, oac, oad, p.ws, tag);
    sfor<iT, NOPS>([&](auto N_) { pin(r[N_]); });
    STAMP(10);
    const float o = rowscatter(w0 * untag(pa) + w1 * untag(pb));
    project4(o, msk2, r[iT], r[iT + 1], r[iT + 2], r[iT + 3], Pub{lw, lr, os, tag, nm, pub, p.ws});
  } else {
    sfor<l0, NOPS>(late);
    v4u ha[6];
    for (int i = 0; i < 6; ++i) { ha[i] = spread(hd[i], msk); pin(ha[i]); }
    v4f s = {0.f, 0.f, 0.f, 0.f};
    pinf(s);
    asm volatile("s_nop 0");
    sfor<0, 5>([&](auto I_) { mma_landed<5 - I_ + (iT + I_ < l0 ? 1 : 0)>(s, ha[I_], r[iT + I_]); });
    STAMP(9);
    project_last(s, ha[5], r[iT + 5], Pub{lw, lr, os, tag, nm, pub, p.ws});
  }
  STAMP(11);
  if constexpr (!A) {
    const int u = (j - 9) * 16 + g;
    if (u < 36 && w == 1) {
      const int cq = (lane & 3) ^ ((lane & 4) ? 3 : 0), pr = lane >> 2;
      const unsigned osf = WS_SF + (unsigned)(u * SF_UNIT + cq * 16);
      unsigned col = u * 16 + cq * 4 + b4 * 2 + hb, fm = pr < 12 ? ~TAGM : 0u;
      unsigned xr = (unsigned)L.x[col] << 16;
      col *= 2;
      unsigned long long lanes = 0x0f0f0f0f00000000ull;
      pinu(xr); pinu(col); pinu(fm);
      asm volatile("" : "+s"(lanes));
      v4u pa, pb;
      poll2(pa, pb, osf + pr * 64, osf + (16 + (pr < 11 ? pr : 11)) * 64, SF_STEP, p.ws, tag);
      STAMP(10);
      finish(pa, pb, nm, fm, asf(xr), col, p.out, lanes);
      if (u == 0 && lane == 0) __hip_atomic_store((unsigned*)(p.ws + WS_EPOCH), pro.epoch + 1, __ATOMIC_RELAXED, __HIP_MEMORY_SCOPE_AGENT);
      STAMP(12);
    }
  }
  if constexpr (A) asm volatile(".LcodeA1:"); else asm volatile(".LcodeB1:");
}

__global__ void __launch_bounds__(512, 2) fused_decode(Params p) {
  __shared__ __attribute__((aligned(16))) Lds L;
  Pro pro;
  STAMP(0);
  const int lane = threadIdx.x & 63, w = __builtin_amdgcn_readfirstlane(threadIdx.x >> 6);
  const int b = blockIdx.x, s = b >> 3, g = 2 * (b & 7) + (s & 1), j = s >> 1;
  const bool A = j < 9;
  const int c = lane & 15, g4 = lane >> 4, c8 = lane & 7;
  v4u xa; unsigned xt, junk0;
  asm volatile("global_load_dwordx4 %0, %1, %2" : "=v"(xa) : "v"(lane * 16), "s"(p.x));
  asm volatile("global_load_ushort %0, %1, %2 offset:1024" : "=v"(xt) : "v"(lane * 2), "s"(p.x));
  asm volatile("global_load_dword %0, %1, %2 sc1" : "=v"(pro.epoch) : "v"(WS_EPOCH), "s"(p.ws));
  {
    unsigned long long pc; unsigned la, lb;
    asm volatile("s_getpc_b64 %0\n.Lcode0:\n\ts_mov_b32 %1, .LcodeA1-.Lcode0\n\ts_mov_b32 %2, .LcodeB1-.Lcode0" : "=s"(pc), "=s"(la), "=s"(lb));
    const unsigned o = min((unsigned)(lane * 8 + w) * 128u, max(la, lb));
    asm volatile("global_load_dword %0, %1, %2" : "=v"(junk0) : "v"(o), "s"(pc));
  }
  asm volatile("s_barrier");
  const int wqrow0 = 72 * (j >> 1) + 9 * w;
  const unsigned owq = (unsigned)(wqrow0 + g4) * 8192u + ((2 * g + (j & 1)) * 128 + 8 * c) * 2;
  const unsigned owq2 = (unsigned)(wqrow0 + 8) * 8192u + ((2 * g + (j & 1)) * 128 + 8 * c) * 2;
  v4u wq0, wq1, wq2;
  ld_nt<0>(wq0, owq, p.wq); ld_nt<0>(wq1, owq + 4 * 8192, p.wq); ld_nt<0>(wq2, owq2, p.wq);
  asm volatile("s_barrier");
  static_assert(WIN >= 3 && WIN <= 9 && QPOS >= 9 && P0 >= WIN, "first window");
  unsigned o0, ostep; const char* base0;
  if (A) {
    const int aidx = gutask(g, j);
    o0 = (unsigned)(144 * (aidx & 3) + 36 * (w & 3) + g4) * (F * 2) + ((aidx >> 2) * 128 + 8 * c) * 2;
    ostep = 4 * F * 2; base0 = w >> 2 ? p.wu : p.wg;
  } else {
    o0 = kvmap(false, lane, w, g, j).o;
    ostep = KVSTEP; base0 = p.k;
  }
  v4u r0[WIN];
  auto first = [&](auto N_) { ld_nt<0>(r0[N_], o0 + N_ * ostep, base0); };
  sfor<0, WIN - 3>(first);
  for (int i = 0; i < 4; ++i) pro.msk[i] = (i == (c8 >> 1)) ? ((lane & 1) ? 0xffff0000u : 0x0000ffffu) : 0u;

  land<WIN + 2>(xa);
  asm volatile("s_waitcnt vmcnt(%1)" : "+v"(xt) : "n"(WIN + 2));
  pro.t1 = (unsigned)__builtin_amdgcn_s_memrealtime();
  STAMP(1);
  *(v4u*)&L.x[lane * 8] = xa; L.x[512 + lane] = (unsigned short)xt;
  const unsigned u0 = L.x[wqrow0 + g4], u1 = L.x[wqrow0 + 4 + g4], u2 = L.x[wqrow0 + 8];
  {
    float xv[9];
    for (int i = 0; i < 4; ++i) { xv[2 * i] = asf(xa[i] << 16); xv[2 * i + 1] = asf(xa[i] & 0xffff0000u); }
    xv[8] = asf(xt << 16);
    const float mean = wavesum(((xv[0] + xv[1]) + (xv[2] + xv[3])) + ((xv[4] + xv[5]) + (xv[6] + xv[7])) + xv[8]) * (1.f / D);
    float s0 = 0.f, s1 = 0.f;
    for (int i = 0; i < 4; ++i) { const float a = xv[2 * i] - mean, b = xv[2 * i + 1] - mean; s0 += a * a; s1 += b * b; }
    const float t = xv[8] - mean;
    const float var = rbf(wavesum(s0 + s1 + t * t) * (1.f / D));
    pro.mb = rbf(mean);
    pro.rden = __builtin_amdgcn_rcpf(rbf(__builtin_amdgcn_sqrtf(rbf(var + 1e-5f))));
  }
  {
    const float y0 = rbf(asf(u0 << 16) - pro.mb) * pro.rden, y1 = rbf(asf(u1 << 16) - pro.mb) * pro.rden;
    const float y2 = g4 == 0 ? rbf(asf(u2 << 16) - pro.mb) * pro.rden : 0.f;
    const unsigned x0 = pk(y0, y0), x1 = pk(y1, y1), x2 = pk(y2, y2);
    land<WIN - 3>(wq2);
    asm volatile("" : "+v"(wq0), "+v"(wq1), "+v"(pro.epoch), "+v"(junk0));
    sfor<WIN - 3, WIN>(first);
    STAMP(15);
    v4f a = {0.f, 0.f, 0.f, 0.f};
    a = mac(a, x0, pro.msk, wq0); a = mac(a, x1, pro.msk, wq1); a = mac(a, x2, pro.msk, wq2);
    if (lane < 32) *(v4f*)&L.rq[w][8 * c + 4 * g4] = a;
    wg_barrier();
    if (w == 0 && lane < 32) {
      v4f s = *(v4f*)&L.rq[0][4 * lane];
      for (int i = 1; i < 8; ++i) s += *(v4f*)&L.rq[i][4 * lane];
      st_dev(WS_QP + (unsigned)((g * 16 + j) * 512 + lane * 16), tagged(s, (pro.epoch + 1) & TAGM), p.ws);
    }
    fence_sched();
  }
  STAMP(2);
  if (A) body<true>(p, L, pro, r0, o0, lane, w, g, j);
  else body<false>(p, L, pro, r0, o0, lane, w, g, j);
}

extern "C" void kernel_launch(void* const* d_in, const int* in_sizes, int n_in, void* d_out, int out_size,
                              void* d_ws, size_t ws_size, hipStream_t stream) {
  Params p;
  p.x = (const char*)d_in[0]; p.k = (const char*)d_in[1]; p.v = (const char*)d_in[2]; p.wq = (const char*)d_in[3];
  p.wo = (const char*)d_in[4]; p.wg = (const char*)d_in[6]; p.wu = (const char*)d_in[7]; p.wd = (const char*)d_in[8];
  p.ws = (char*)d_ws; p.out = (char*)d_out;
  fused_decode<<<256, 512, 0, stream>>>(p);
}
```
